```python
import torch

def _softmax(x, axis=-1):
    return torch.softmax(x, dim=axis)
def _gelu(x):
    return torch.nn.functional.gelu(x, approximate="tanh")
def _silu(x):
    return torch.nn.functional.silu(x)
def _sigmoid(x):
    return torch.sigmoid(x)
def _layernorm(x, axis=-1, eps=1e-5):
    m = x.mean(dim=axis, keepdim=True)
    v = x.var(dim=axis, keepdim=True, unbiased=False)
    return (x - m) / torch.sqrt(v + eps)
def _scatter_add(tgt, idx, src, axis=0):
    return tgt.clone().index_add_(axis, idx, src)
def _segment_sum(src, seg, n):
    out = torch.zeros((n,) + src.shape[1:], dtype=src.dtype)
    return out.index_add_(0, seg, src)
def _pad(x, pw):
    flat = tuple(v for pair in reversed(pw) for v in pair)
    return torch.nn.functional.pad(x, flat)

kernel_name = 'interleave_layers__sliding_block_cached__mixer_token_mlp__decode'

def setup_inputs():

    _w = (lambda a: (lambda t: t.to(torch.bfloat16) if t.is_floating_point() else t)(torch.as_tensor(a)))
    def _A_setup():
        B, Sq, D = 1, 1, 512
        Hq, Hkv, Sk, Dh = 32, 8, 65536, 128
        assert Hq % Hkv == 0, (Hq, Hkv)
        return {
            "x": _w((lambda *s: torch.randn(*s, dtype=torch.bfloat16) * 0.05)(B, Sq, D)),
            "k_cache": _w((lambda *s: torch.randn(*s, dtype=torch.bfloat16) * 0.8)(B, Hkv, Sk, Dh)),
            "v_cache": _w((lambda *s: torch.randn(*s, dtype=torch.bfloat16) * 1.6)(B, Hkv, Sk, Dh)),
            "wq": _w((lambda *s: torch.randn(*s, dtype=torch.bfloat16) * 0.8)(D, Hq * Dh) * (D ** -0.5)),
            "wo": _w((lambda *s: torch.randn(*s, dtype=torch.bfloat16) * 1.6)(Hq * Dh, D) * ((Hq * Dh) ** -0.5)),
        }
    _A_in = _A_setup()

    def _B_setup():
        B, S, D, F = 1, 1, 512, 1792
        return {
            "x": _w((lambda *s: torch.randn(*s, dtype=torch.bfloat16) * 0.05)(B, S, D)),
            "w1": _w((lambda *s: torch.randn(*s, dtype=torch.bfloat16) * 0.8)(S, F) * (S ** -0.5)),
            "b1": _w((lambda *s: torch.randn(*s, dtype=torch.bfloat16) * 0.2)(F) * 0.02),
            "w2": _w((lambda *s: torch.randn(*s, dtype=torch.bfloat16) * 0.2)(F, S) * (F ** -0.5)),
            "b2": _w((lambda *s: torch.randn(*s, dtype=torch.bfloat16) * 0.2)(S) * 0.02),
        }
    _B_in = _B_setup()

    return {
        "A_x": _A_in["x"],
        "A_k_cache": _A_in["k_cache"],
        "A_v_cache": _A_in["v_cache"],
        "A_wq": _A_in["wq"],
        "A_wo": _A_in["wo"],
        "B_x": _B_in["x"],
        "B_w1": _B_in["w1"],
        "B_b1": _B_in["b1"],
        "B_w2": _B_in["w2"],
        "B_b2": _B_in["b2"],
    }

def reference(A_x, A_k_cache, A_v_cache, A_wq, A_wo, B_x, B_w1, B_b1, B_w2, B_b2):
    def _A_ref(x, k_cache, v_cache, wq, wo):
        B, Sq, D = x.shape
        Hq, Dh = 32, 128
        Hkv, Sk = k_cache.shape[1], k_cache.shape[2]
        W = min(4096, max(256, Sk // 4))
        rep = Hq // Hkv
        q = (lambda x, *d: x.permute(*d))((x @ wq).reshape(B, Sq, Hq, Dh), 0, 2, 1, 3)
        k = (lambda x, n, axis: torch.repeat_interleave(x, n, dim=axis))(k_cache[:, :, Sk - W:, :], rep, axis=1)
        v = (lambda x, n, axis: torch.repeat_interleave(x, n, dim=axis))(v_cache[:, :, Sk - W:, :], rep, axis=1)
        scores = torch.einsum("bhqd,bhkd->bhqk", q, k) * (Dh ** -0.5)
        p = _softmax(scores, axis=-1)
        o = torch.einsum("bhqk,bhkd->bhqd", p, v)
        return (lambda x, *d: x.permute(*d))(o, 0, 2, 1, 3).reshape(B, Sq, Hq * Dh) @ wo

    def _B_ref(x, w1, b1, w2, b2):
        B, S, D = x.shape
        h = _layernorm(x, axis=-1)
        u = torch.einsum("bsd,sf->bdf", h, w1) + b1
        u = _gelu(u)
        y = torch.einsum("bdf,fs->bds", u, w2) + b2
        return (lambda x, *d: x.permute(*d))(y, 0, 2, 1)

    x = A_x
    x = x + _A_ref(_layernorm(x, axis=-1), A_k_cache, A_v_cache, A_wq, A_wo)
    x = x + _B_ref(_layernorm(x, axis=-1), B_w1, B_b1, B_w2, B_b2)
    x = x + _A_ref(_layernorm(x, axis=-1), A_k_cache, A_v_cache, A_wq, A_wo)
    x = x + _B_ref(_layernorm(x, axis=-1), B_w1, B_b1, B_w2, B_b2)
    return x
```

```cpp
#include <hip/hip_runtime.h>
#include <hip/hip_bf16.h>
#include <stdint.h>

typedef unsigned short u16;
__device__ __forceinline__ float bf2f(u16 b) { return __builtin_bit_cast(float, (unsigned)b << 16); }
__device__ __forceinline__ float bfr(float x) { return (float)(__bf16)x; }
__device__ __forceinline__ u16 f2bf(float x) { return __builtin_bit_cast(u16, (__bf16)x); }

constexpr int D = 512, HQ = 32, HKV = 8, SK = 65536, DH = 128, W = 4096, F = 1792;
constexpr int NSPLIT = 32;

__device__ void ln_block(const float* xin, float* out, float* red) {
    const int t = threadIdx.x;
    float a = xin[t], b = xin[t + 256];
    float s = a + b;
    for (int o = 32; o >= 1; o >>= 1) s += __shfl_xor(s, o);
    if ((t & 63) == 0) red[t >> 6] = s;
    __syncthreads();
    float tot = red[0] + red[1] + red[2] + red[3];
    __syncthreads();
    float mean = tot * (1.0f / 512.0f);
    float da = a - mean, db = b - mean;
    float v = da * da + db * db;
    for (int o = 32; o >= 1; o >>= 1) v += __shfl_xor(v, o);
    if ((t & 63) == 0) red[t >> 6] = v;
    __syncthreads();
    float vt = red[0] + red[1] + red[2] + red[3];
    __syncthreads();
    float var = bfr(vt * (1.0f / 512.0f));
    float m = bfr(mean);
    float den = bfr(sqrtf(bfr(var + 1e-5f)));
    out[t] = bfr(bfr(a - m) / den);
    out[t + 256] = bfr(bfr(b - m) / den);
    __syncthreads();
}

__global__ void k_q(const float* x, const u16* wq, float* q) {
    __shared__ float xs[512], hs[512], red[4];
    const int t = threadIdx.x;
    xs[t] = x[t]; xs[t + 256] = x[t + 256];
    __syncthreads();
    ln_block(xs, hs, red);
    const int c = blockIdx.x * 256 + t;
    float acc = 0.f;
    for (int i = 0; i < 512; ++i) acc += hs[i] * bf2f(wq[(size_t)i * 4096 + c]);
    q[c] = bfr(acc);
}

__global__ void k_attn(const float* q, const u16* kc, const u16* vc, float* po, float* pml) {
    __shared__ float qs[4][128], ps[4][128], red[4][2];
    const int s = blockIdx.x, g = blockIdx.y, t = threadIdx.x;
    for (int h = 0; h < 4; ++h) qs[h][t] = q[(g * 4 + h) * 128 + t];
    __syncthreads();
    const size_t base = ((size_t)g * SK + (SK - W) + s * 128) * DH;
    float sc[4] = {0, 0, 0, 0};
    for (int d = 0; d < 128; ++d) {
        float kv = bf2f(kc[base + (size_t)t * DH + d]);
        for (int h = 0; h < 4; ++h) sc[h] += qs[h][d] * kv;
    }
    float mx[4], l[4];
    for (int h = 0; h < 4; ++h) {
        sc[h] = bfr(bfr(sc[h]) * 0.08838834764831845f);
        float m = sc[h];
        for (int o = 32; o >= 1; o >>= 1) m = fmaxf(m, __shfl_xor(m, o));
        if ((t & 63) == 0) red[h][t >> 6] = m;
    }
    __syncthreads();
    for (int h = 0; h < 4; ++h) mx[h] = fmaxf(red[h][0], red[h][1]);
    __syncthreads();
    for (int h = 0; h < 4; ++h) {
        float p = __expf(sc[h] - mx[h]);
        ps[h][t] = p;
        float ss = p;
        for (int o = 32; o >= 1; o >>= 1) ss += __shfl_xor(ss, o);
        if ((t & 63) == 0) red[h][t >> 6] = ss;
    }
    __syncthreads();
    for (int h = 0; h < 4; ++h) l[h] = red[h][0] + red[h][1];
    float o[4] = {0, 0, 0, 0};
    for (int j = 0; j < 128; ++j) {
        float vv = bf2f(vc[base + (size_t)j * DH + t]);
        for (int h = 0; h < 4; ++h) o[h] += ps[h][j] * vv;
    }
    for (int h = 0; h < 4; ++h) {
        int hh = g * 4 + h;
        po[((size_t)hh * NSPLIT + s) * 128 + t] = o[h];
        if (t == 0) { pml[(hh * NSPLIT + s) * 2] = mx[h]; pml[(hh * NSPLIT + s) * 2 + 1] = l[h]; }
    }
}

__global__ void k_comb(const float* po, const float* pml, float* o) {
    const int h = blockIdx.x, t = threadIdx.x;
    float M = -INFINITY;
    for (int s = 0; s < NSPLIT; ++s) M = fmaxf(M, pml[(h * NSPLIT + s) * 2]);
    float L = 0.f, acc = 0.f;
    for (int s = 0; s < NSPLIT; ++s) {
        float w = __expf(pml[(h * NSPLIT + s) * 2] - M);
        L += w * pml[(h * NSPLIT + s) * 2 + 1];
        acc += w * po[((size_t)h * NSPLIT + s) * 128 + t];
    }
    o[h * 128 + t] = bfr(acc / L);
}

__global__ void k_wo(const float* o, const u16* wo, float* part) {
    const int c = blockIdx.x, t = threadIdx.x;
    float acc = 0.f;
    for (int i = 0; i < 128; ++i) acc += o[c * 128 + i] * bf2f(wo[(size_t)(c * 128 + i) * 512 + t]);
    part[c * 512 + t] = acc;
}

__global__ void k_x1(const float* part, float* x, float* h2) {
    __shared__ float xs[512], h1[512], hs[512], red[4];
    const int t = threadIdx.x;
    for (int j = 0; j < 2; ++j) {
        int d = t + 256 * j;
        float acc = 0.f;
        for (int c = 0; c < 32; ++c) acc += part[c * 512 + d];
        float v = bfr(x[d] + bfr(acc));
        xs[d] = v; x[d] = v;
    }
    __syncthreads();
    ln_block(xs, h1, red);
    ln_block(h1, hs, red);
    h2[t] = hs[t]; h2[t + 256] = hs[t + 256];
}

__device__ __forceinline__ float gelu_tanh(float u) {
    float z = 0.7978845608028654f * (u + 0.044715f * u * u * u);
    return 0.5f * u * (1.0f + tanhf(z));
}

__global__ void k_mix(const float* h2, const u16* w1, const u16* b1, const u16* w2, const u16* b2, float* x, u16* out, int last) {
    __shared__ float red[4];
    const int d = blockIdx.x, t = threadIdx.x;
    float h = h2[d];
    float acc = 0.f;
    for (int f = t; f < F; f += 256) {
        float u = bfr(bfr(h * bf2f(w1[f])) + bf2f(b1[f]));
        acc += bfr(gelu_tanh(u)) * bf2f(w2[f]);
    }
    for (int o = 32; o >= 1; o >>= 1) acc += __shfl_xor(acc, o);
    if ((t & 63) == 0) red[t >> 6] = acc;
    __syncthreads();
    if (t == 0) {
        float y = bfr(bfr(red[0] + red[1] + red[2] + red[3]) + bf2f(b2[0]));
        float v = bfr(x[d] + y);
        x[d] = v;
        if (last) out[d] = f2bf(v);
    }
}

__global__ void k_init(const u16* xin, float* x) { x[threadIdx.x] = bf2f(xin[threadIdx.x]); }

extern "C" void kernel_launch(void* const* d_in, const int* in_sizes, int n_in,
                              void* d_out, int out_size, void* d_ws, size_t ws_size,
                              hipStream_t stream) {
    const u16* x = (const u16*)d_in[0];
    const u16* kc = (const u16*)d_in[1];
    const u16* vc = (const u16*)d_in[2];
    const u16* wq = (const u16*)d_in[3];
    const u16* wo = (const u16*)d_in[4];
    const u16* w1 = (const u16*)d_in[6];
    const u16* b1 = (const u16*)d_in[7];
    const u16* w2 = (const u16*)d_in[8];
    const u16* b2 = (const u16*)d_in[9];
    float* ws = (float*)d_ws;
    float* xf = ws;
    float* q = ws + 512;
    float* o = q + 4096;
    float* h2 = o + 4096;
    float* pml = h2 + 512;
    float* po = pml + 2048;
    float* part = po + 131072;
    k_init<<<1, 512, 0, stream>>>(x, xf);
    for (int layer = 0; layer < 2; ++layer) {
        k_q<<<16, 256, 0, stream>>>(xf, wq, q);
        k_attn<<<dim3(NSPLIT, HKV), 128, 0, stream>>>(q, kc, vc, po, pml);
        k_comb<<<32, 128, 0, stream>>>(po, pml, o);
        k_wo<<<32, 512, 0, stream>>>(o, wo, part);
        k_x1<<<1, 256, 0, stream>>>(part, xf, h2);
        k_mix<<<512, 256, 0, stream>>>(h2, w1, b1, w2, b2, xf, (u16*)d_out, layer == 1);
    }
}
```

```cpp
#include <hip/hip_runtime.h>
#include <hip/hip_bf16.h>
#include <stdint.h>

typedef unsigned short u16;
typedef unsigned v4u __attribute__((ext_vector_type(4)));
typedef unsigned v2u __attribute__((ext_vector_type(2)));
typedef float f4 __attribute__((ext_vector_type(4)));
typedef float f2 __attribute__((ext_vector_type(2)));
typedef short s4 __attribute__((ext_vector_type(4)));
typedef __bf16 b8 __attribute__((ext_vector_type(8)));
typedef __bf16 b2 __attribute__((ext_vector_type(2)));
#define LAS __attribute__((address_space(3)))
#define GAS __attribute__((address_space(1)))

#define D_EB 10
#define D_EC 10
#define D_EA 12
#define STAMPS 0

namespace fz {

constexpr int SK = 65536, WIN = 4096, NF = 1792;
constexpr unsigned INV = 0xFFFFFFFFu;
constexpr int WS_R = 0;
constexpr int WS_TAB = 4096;
constexpr int WS_EB = 16384;
constexpr int WS_EC = WS_EB + 3 * 131072;
constexpr int WS_EL = WS_EC + 2 * 262144;
constexpr int WS_EA = WS_EL + 2 * 4096;
constexpr int WS_END = WS_EA + 2 * 16384;
constexpr int WS_STAMP = 1 << 20;
constexpr int L_STG = 0;
constexpr int L_SQ = 32768;
constexpr int L_SO = L_SQ + 16384;
constexpr int L_TAB = L_SO + 16384;
constexpr int L_OW = L_TAB + 8192;
constexpr int L_QS = L_OW + 8 * 4 * 132 * 4;
constexpr int L_XB = L_QS + 1024;
constexpr int L_XS = L_XB + 1024;
constexpr int L_M = L_XS + 2048;
constexpr int L_L = L_M + 128;
constexpr int L_WR = L_L + 128;
constexpr int L_ST1 = L_WR + 512;
constexpr int L_ST2 = L_ST1 + 64;
constexpr int L_TR = L_ST2 + 64;
constexpr int L_END = L_TR + 512;
constexpr int LDS_BYTES = 100 * 1024;
static_assert(L_END <= LDS_BYTES, "lds");
constexpr int TE0 = 115;
constexpr int SPIN_CAP = 1 << 22;

__device__ __forceinline__ float u2f(unsigned u) { return __builtin_bit_cast(float, u); }
__device__ __forceinline__ unsigned f2u(float f) { return __builtin_bit_cast(unsigned, f); }
__device__ __forceinline__ float bflo(unsigned u) { return u2f(u << 16); }
__device__ __forceinline__ float bfhi(unsigned u) { return u2f(u & 0xffff0000u); }
__device__ __forceinline__ float bfr(float x) { return (float)(__bf16)x; }
__device__ __forceinline__ unsigned pk(float lo, float hi) { b2 v = {(__bf16)lo, (__bf16)hi}; return __builtin_bit_cast(unsigned, v); }
__device__ __forceinline__ unsigned nn(unsigned w) { return w == INV ? 0x7fc07fc0u : w; }
__device__ __forceinline__ f4 mfma32(v4u a, v4u b, f4 c) { return __builtin_amdgcn_mfma_f32_16x16x32_bf16(__builtin_bit_cast(b8, a), __builtin_bit_cast(b8, b), c, 0, 0, 0); }
__device__ __forceinline__ f4 mfma16(v2u a, v2u b, f4 c) { return __builtin_amdgcn_mfma_f32_16x16x16bf16_1k(__builtin_bit_cast(s4, a), __builtin_bit_cast(s4, b), c, 0, 0, 0); }
__device__ __forceinline__ v2u trd(LAS unsigned char* p) { return __builtin_bit_cast(v2u, __builtin_amdgcn_ds_read_tr16_b64_v4i16((LAS s4*)p)); }
template <int CTRL> __device__ __forceinline__ float dpp(float x) { return u2f(__builtin_amdgcn_mov_dpp(f2u(x), CTRL, 0xf, 0xf, true)); }
template <int CTRL> __device__ __forceinline__ unsigned dppu(unsigned x) { return (unsigned)__builtin_amdgcn_mov_dpp((int)x, CTRL, 0xf, 0xf, true); }
constexpr int ROR8 = 0x128, ROR4 = 0x124, ROR2 = 0x122, ROR1 = 0x121;
__device__ __forceinline__ float red32(float a, float b) { auto r = __builtin_amdgcn_permlane32_swap(f2u(a), f2u(b), false, false); return u2f(r[0]) + u2f(r[1]); }
__device__ __forceinline__ float red16(float a, float b) { auto r = __builtin_amdgcn_permlane16_swap(f2u(a), f2u(b), false, false); return u2f(r[0]) + u2f(r[1]); }
__device__ __forceinline__ float max32(float a) { auto r = __builtin_amdgcn_permlane32_swap(f2u(a), f2u(a), false, false); return fmaxf(u2f(r[0]), u2f(r[1])); }
__device__ __forceinline__ float max16(float a) { auto r = __builtin_amdgcn_permlane16_swap(f2u(a), f2u(a), false, false); return fmaxf(u2f(r[0]), u2f(r[1])); }
__device__ __forceinline__ float rowsum(float x) { x += dpp<ROR8>(x); x += dpp<ROR4>(x); x += dpp<ROR2>(x); x += dpp<ROR1>(x); return x; }
#define BAR() asm volatile("s_waitcnt lgkmcnt(0)\n\ts_barrier" ::: "memory")
#define CFENCE() asm volatile("" ::: "memory")
#define SB() __builtin_amdgcn_sched_barrier(0)
template <int N> __device__ __forceinline__ void nap() {
#pragma unroll
    for (int i = 0; i < N / 8; ++i) __builtin_amdgcn_s_sleep(8);
    if (N % 8) __builtin_amdgcn_s_sleep(N % 8);
}
__device__ __forceinline__ bool vok(v4u a) { return a.x != INV && a.y != INV && a.z != INV && a.w != INV; }

__device__ __forceinline__ float gelu(float u) {
    const float c0 = -2.0f * 0.7978845608028654f * 1.4426950408889634f, c1 = c0 * 0.044715f;
    float e = __builtin_amdgcn_exp2f(u * fmaf(u * u, c1, c0));
    return u * __builtin_amdgcn_rcpf(1.0f + e);
}
__device__ __forceinline__ float mix2(float h, float w1a, float w1b, float b1a, float b1b, unsigned w2p, float acc) {
    unsigned m = pk(h * w1a, h * w1b);
    unsigned u = pk(bflo(m) + b1a, bfhi(m) + b1b);
    unsigned gq = pk(gelu(bflo(u)), gelu(bfhi(u)));
    return __builtin_amdgcn_fdot2_f32_bf16(__builtin_bit_cast(b2, gq), __builtin_bit_cast(b2, w2p), acc, false);
}

struct Args {
    const u16 *x, *kc, *vc, *wq, *wo, *w1, *b1, *w2, *b2;
    u16* out;
    unsigned char* ws;
};

#define STAMP(i) do {} while (0)

__global__ void __launch_bounds__(512, 2) fused(Args a) {
    extern __shared__ __attribute__((aligned(16))) unsigned char smem[];
    LAS unsigned char* L = (LAS unsigned char*)smem;
    const int b = blockIdx.x, tid = threadIdx.x;
    const int w = __builtin_amdgcn_readfirstlane(tid >> 6), lane = tid & 63;
    const int g = b & 7, r = b >> 3;
    const int kb = r & 7, hq = r >> 3, hp = r >> 4, cb = r & 15;
    const int n = lane & 15, q = lane >> 4;
    const __amdgpu_buffer_rsrc_t rs = __builtin_amdgcn_make_buffer_rsrc((void*)a.ws, 0, WS_END, 0x00020000);
    GAS unsigned* Rw = (GAS unsigned*)a.ws;
    auto ld16 = [&](int off) -> v4u { return __builtin_amdgcn_raw_buffer_load_b128(rs, off, 0, 16); };
    auto ld4 = [&](int off) -> unsigned { return __builtin_amdgcn_raw_buffer_load_b32(rs, off, 0, 16); };
    auto st16 = [&](int off, v4u v) { __builtin_amdgcn_raw_buffer_store_b128(v, rs, off, 0, 16); };
    auto st4 = [&](int off, unsigned v) { __builtin_amdgcn_raw_buffer_store_b32(v, rs, off, 0, 16); };
    auto bump = [&]() { if (lane == 0) __hip_atomic_fetch_add(Rw + b, 1u, __ATOMIC_RELAXED, __HIP_MEMORY_SCOPE_AGENT); };
    STAMP(0);

    const size_t kvb = ((size_t)g * SK + (SK - WIN) + r * 128 + w * 16) * 128;
    const __amdgpu_buffer_rsrc_t rk = __builtin_amdgcn_make_buffer_rsrc((void*)(a.kc + kvb), 0, 4096, 0x00020000);
    const __amdgpu_buffer_rsrc_t rv = __builtin_amdgcn_make_buffer_rsrc((void*)(a.vc + kvb), 0, 4096, 0x00020000);
    const __amdgpu_buffer_rsrc_t rq = __builtin_amdgcn_make_buffer_rsrc((void*)(a.wq + (size_t)kb * 64 * 4096 + (g * 4 + hq) * 128), 0, 64 * 8192 - 7936, 0x00020000);
    const __amdgpu_buffer_rsrc_t ro = __builtin_amdgcn_make_buffer_rsrc((void*)(a.wo + (size_t)(g * 2 + hp) * 256 * 512 + cb * 32), 0, 256 * 1024 - 960, 0x00020000);

    v4u snap = {0, 0, 0, 0};
    v4u wqs[4];
    if (w == 7) {
        snap = ld16(WS_R + lane * 16);
        const v4u iv = {INV, INV, INV, INV};
        st16(WS_EC + g * 32768 + (lane >> 2) * 2048 + r * 64 + (lane & 3) * 16, iv);
        st16(WS_EC + 262144 + g * 32768 + (lane >> 2) * 2048 + r * 64 + (lane & 3) * 16, iv);
        st16(WS_EB + (lane >> 5) * 131072 + g * 16384 + kb * 2048 + hq * 512 + (lane & 31) * 16, iv);
        int o;
        if (lane < 32) o = WS_EB + 2 * 131072 + g * 16384 + kb * 2048 + hq * 512 + lane * 16;
        else if (lane < 40) o = WS_EA + ((lane >> 2) & 1) * 16384 + (g * 2 + hp) * 1024 + cb * 64 + (lane & 3) * 16;
        else if (lane < 42) o = WS_EL + (lane & 1) * 4096 + g * 512 + r * 16;
        else o = WS_TAB + b * 32 + (lane & 1) * 16;
        if (lane < 44) st16(o, iv);
    } else if (w < 4) {
#pragma unroll
        for (int i = 0; i < 4; ++i) wqs[i] = __builtin_amdgcn_raw_buffer_load_b128(rq, (w * 16 + i * 4 + q) * 8192 + n * 16, 0, 2);
    }
    CFENCE();
    const v4u x0v = *(const v4u*)(a.x + lane * 8);
    v4u xf[2];
#pragma unroll
    for (int ks = 0; ks < 2; ++ks) xf[ks] = *(const v4u*)(a.x + kb * 64 + ks * 32 + q * 8);
    v2u p1 = {0, 0}, pb = {0, 0}, p2 = {0, 0};
    if (w < 7) { p1 = *(const v2u*)(a.w1 + tid * 4); pb = *(const v2u*)(a.b1 + tid * 4); p2 = *(const v2u*)(a.w2 + tid * 4); }
    const float b2v = bflo(a.b2[0]);
    float mu, rsig;
    {
        float xv[8] = {bflo(x0v.x), bfhi(x0v.x), bflo(x0v.y), bfhi(x0v.y), bflo(x0v.z), bfhi(x0v.z), bflo(x0v.w), bfhi(x0v.w)};
        float s = 0.f;
#pragma unroll
        for (int j = 0; j < 8; ++j) s += xv[j];
        s = red32(s, s); s = red16(s, s); s = rowsum(s);
        mu = s * (1.0f / 512.0f);
        float v = 0.f;
#pragma unroll
        for (int j = 0; j < 8; ++j) v = fmaf(xv[j] - mu, xv[j] - mu, v);
        v = red32(v, v); v = red16(v, v); v = rowsum(v);
        rsig = __builtin_amdgcn_rsqf(v * (1.0f / 512.0f) + 1e-5f);
        if (w == 4) {
            *(LAS f4*)(L + L_XS + lane * 32) = f4{xv[0], xv[1], xv[2], xv[3]};
            *(LAS f4*)(L + L_XS + lane * 32 + 16) = f4{xv[4], xv[5], xv[6], xv[7]};
        }
    }
    if (n == 1) { xf[0] = v4u{0x3f803f80u, 0x3f803f80u, 0x3f803f80u, 0x3f803f80u}; xf[1] = xf[0]; }

    v4u kst[4];
    float tacc[16];
    const float w1f[4] = {bflo(p1.x), bfhi(p1.x), bflo(p1.y), bfhi(p1.y)}, b1f[4] = {bflo(pb.x), bfhi(pb.x), bflo(pb.y), bfhi(pb.y)};
    const unsigned hbits0 = (unsigned)(((b >> 3) & 1) << 15) | (unsigned)((TE0 + (b >> 4)) << 7) | (unsigned)((b & 7) * 16);
    auto tab_entry = [&](int e) {
        const float h = u2f((hbits0 + e) << 16);
        float acc = mix2(h, w1f[0], w1f[1], b1f[0], b1f[1], p2.x, 0.f);
        tacc[e] = mix2(h, w1f[2], w1f[3], b1f[2], b1f[3], p2.y, acc);
    };
    auto issue_k = [&]() {
#pragma unroll
        for (int i = 0; i < 4; ++i) kst[i] = __builtin_amdgcn_raw_buffer_load_b128(rk, i * 1024 + lane * 16, 0, 2);
        SB();
    };
    if (w == 7) {
        asm volatile("s_waitcnt vmcnt(0)" ::: "memory");
        bump();
    } else {
#pragma unroll
        for (int e = 0; e < 3; ++e) tab_entry(e);
        SB();
        issue_k();
#pragma unroll
        for (int e = 3; e < 6; ++e) tab_entry(e);
        SB();
        if (w < 4) {
#pragma unroll
            for (int i = 0; i < 4; ++i) { const int row = w * 16 + i * 4 + q;
                *(LAS v4u*)(L + L_SQ + row * 256 + ((n ^ (((row & 3) << 2) | ((row >> 2) & 3))) * 16)) = wqs[i]; }
        }
    }
    STAMP(1);
    BAR();
    v4u wqf[2];
    {
        const int aa = n >> 2, p = n & 3;
#pragma unroll
        for (int ks = 0; ks < 2; ++ks) {
            v2u t[2];
#pragma unroll
            for (int tt = 0; tt < 2; ++tt) { const int row = ks * 32 + q * 8 + tt * 4 + aa, ch = 2 * w + (p >> 1);
                t[tt] = trd(L + L_SQ + row * 256 + ((ch ^ (((row & 3) << 2) | ((row >> 2) & 3))) * 16) + 8 * (p & 1)); }
            wqf[ks] = v4u{t[0].x, t[0].y, t[1].x, t[1].y};
        }
        f4 c = {0, 0, 0, 0};
        c = mfma32(wqf[0], xf[0], c); c = mfma32(wqf[1], xf[1], c);
        if (n < 2) st16(WS_EB + n * 2 * 131072 + g * 16384 + kb * 2048 + hq * 512 + w * 64 + q * 16, v4u{nn(f2u(c.x)), nn(f2u(c.y)), nn(f2u(c.z)), nn(f2u(c.w))});
    }
    STAMP(2);
    if (w == 7) {
        nap<16>();
        for (int it = 0;; ++it) {
            CFENCE();
            const v4u cur = ld16(WS_R + lane * 16);
            if (__all(cur.x != snap.x && cur.y != snap.y && cur.z != snap.z && cur.w != snap.w)) break;
            if ((it & 3) == 3) bump();
            if (it > SPIN_CAP) break;
            nap<8>();
        }
        CFENCE();
        issue_k();
    } else {
#pragma unroll
        for (int e = 6; e < 16; ++e) tab_entry(e);
        float t8[8], t4[4];
#pragma unroll
        for (int j = 0; j < 8; ++j) t8[j] = red32(tacc[j], tacc[j + 8]);
#pragma unroll
        for (int j = 0; j < 4; ++j) t4[j] = rowsum(red16(t8[j], t8[j + 4]));
        if (n == 0) *(LAS f4*)(L + L_TR + w * 64 + q * 16) = f4{t4[0], t4[1], t4[2], t4[3]};
    }
    STAMP(3);
    BAR();
    if (w == 6 && lane < 8) {
        float s0 = 0.f, s1 = 0.f;
#pragma unroll
        for (int ww = 0; ww < 7; ++ww) { const f2 v = *(LAS f2*)(L + L_TR + ww * 64 + lane * 8); s0 += v.x; s1 += v.y; }
        st4(WS_TAB + b * 32 + lane * 4, nn(pk(bfr(s0) + b2v, bfr(s1) + b2v)));
    }

    v4u kf[4];
    v2u vf[8];
    v4u wof[2][2];
    float csum = 0.f;
    LAS unsigned char* stg = L + L_STG + w * 4096;

#pragma unroll
    for (int layer = 0; layer < 2; ++layer) {
        {
            const int o0 = WS_EB + layer * 131072 + g * 16384 + q * 2048 + w * 256 + n * 16;
            v4u e0, e1, c0, c1;
            if (layer == 1) nap<D_EB>();
            for (int it = 0;; ++it) {
                CFENCE();
                e0 = ld16(o0); e1 = ld16(o0 + 8192);
                if (layer == 0) { c0 = ld16(o0 + 2 * 131072); c1 = ld16(o0 + 2 * 131072 + 8192); }
                if (layer == 0 && it == 0) {
#pragma unroll
                    for (int i = 0; i < 4; ++i) { const int row = i * 4 + q; *(LAS v4u*)(stg + row * 256 + ((n ^ row) * 16)) = kst[i]; }
#pragma unroll
                    for (int ks = 0; ks < 4; ++ks) kf[ks] = *(LAS v4u*)(stg + n * 256 + (((ks * 4 + q) ^ n) * 16));
                }
                bool ok = vok(e0) && vok(e1);
                if (layer == 0) ok = ok && vok(c0) && vok(c1);
                if (__all(ok)) break;
                if (layer == 0 && (it & 7) == 7) bump();
                if (it > SPIN_CAP) break;
            }
            CFENCE();
            if (layer == 0) {
#pragma unroll
                for (int i = 0; i < 4; ++i) kst[i] = __builtin_amdgcn_raw_buffer_load_b128(rv, i * 1024 + lane * 16, 0, 2);
                SB();
                csum = red16(red32(u2f(c0.x) + u2f(c1.x), u2f(c0.z) + u2f(c1.z)), red32(u2f(c0.y) + u2f(c1.y), u2f(c0.w) + u2f(c1.w)));
            }
            const float raw = red16(red32(u2f(e0.x) + u2f(e1.x), u2f(e0.z) + u2f(e1.z)), red32(u2f(e0.y) + u2f(e1.y), u2f(e0.w) + u2f(e1.w)));
            const float qv = rsig * (raw - mu * csum);
            *(LAS __bf16*)(L + L_QS + (w * 64 + n * 4 + (q >> 1) * 2 + (q & 1)) * 2) = (__bf16)qv;
        }
        STAMP(4 + layer * 12);
        BAR();
        {
            f4 s = {0, 0, 0, 0};
#pragma unroll
            for (int ks = 0; ks < 4; ++ks) s = mfma32(kf[ks], *(LAS v4u*)(L + L_QS + (n & 3) * 256 + ks * 64 + q * 16), s);
            const float C2 = 0.08838834764831845f * 1.4426950408889634f;
            float m = fmaxf(fmaxf(s.x, s.y), fmaxf(s.z, s.w));
            m = max32(max16(m)) * C2;
            const float e0 = __builtin_amdgcn_exp2f(fmaf(s.x, C2, -m)), e1 = __builtin_amdgcn_exp2f(fmaf(s.y, C2, -m)),
                        e2 = __builtin_amdgcn_exp2f(fmaf(s.z, C2, -m)), e3 = __builtin_amdgcn_exp2f(fmaf(s.w, C2, -m));
            float ls = (e0 + e1) + (e2 + e3);
            ls = red32(ls, ls); ls = red16(ls, ls);
            const v2u pf = {pk(e0, e1), pk(e2, e3)};
            if (lane < 4) { *(LAS float*)(L + L_M + lane * 32 + w * 4) = m; *(LAS float*)(L + L_L + lane * 32 + w * 4) = ls; }
            STAMP(5 + layer * 12);
            if (layer == 0) {
#pragma unroll
                for (int i = 0; i < 4; ++i) { const int row = i * 4 + q; *(LAS v4u*)(stg + row * 256 + ((n ^ ((row & 7) << 1)) * 16)) = kst[i]; }
                if (w >= 4) {
#pragma unroll
                    for (int i = 0; i < 4; ++i) kst[i] = __builtin_amdgcn_raw_buffer_load_b128(ro, (((w - 4) * 4 + i) * 16 + (lane >> 2)) * 1024 + (lane & 3) * 16, 0, 2);
                    SB();
                }
                const int row = q * 4 + (n >> 2), p = n & 3;
#pragma unroll
                for (int c = 0; c < 8; ++c) vf[c] = trd(stg + row * 256 + (((2 * c + (p >> 1)) ^ ((row & 7) << 1)) * 16) + 8 * (p & 1));
            }
            STAMP(6 + layer * 12);
#pragma unroll
            for (int c = 0; c < 8; ++c) {
                const f4 o = mfma16(vf[c], pf, f4{0, 0, 0, 0});
                if (n < 4) *(LAS f4*)(L + L_OW + (w * 4 + n) * 528 + c * 64 + q * 16) = o;
            }
        }
        BAR();
        if (w >= 4) {
            const int h = w - 4;
            const f4 ma = *(LAS f4*)(L + L_M + h * 32), mb = *(LAS f4*)(L + L_M + h * 32 + 16);
            const f4 la = *(LAS f4*)(L + L_L + h * 32), lb = *(LAS f4*)(L + L_L + h * 32 + 16);
            const float M = fmaxf(fmaxf(fmaxf(ma.x, ma.y), fmaxf(ma.z, ma.w)), fmaxf(fmaxf(mb.x, mb.y), fmaxf(mb.z, mb.w)));
            const float mm[8] = {ma.x, ma.y, ma.z, ma.w, mb.x, mb.y, mb.z, mb.w}, ll[8] = {la.x, la.y, la.z, la.w, lb.x, lb.y, lb.z, lb.w};
            float Ls = 0.f, o0 = 0.f, o1 = 0.f;
#pragma unroll
            for (int ww = 0; ww < 8; ++ww) {
                const float e = __builtin_amdgcn_exp2f(mm[ww] - M);
                const f2 v = *(LAS f2*)(L + L_OW + (ww * 4 + h) * 528 + lane * 8);
                Ls = fmaf(e, ll[ww], Ls); o0 = fmaf(e, v.x, o0); o1 = fmaf(e, v.y, o1);
            }
            const float inv = __builtin_amdgcn_rcpf(Ls);
            const unsigned wd = nn(pk(o0 * inv, o1 * inv));
            const v4u v = {wd, dppu<0x55>(wd), dppu<0xAA>(wd), dppu<0xFF>(wd)};
            if ((lane & 3) == 0) st16(WS_EC + layer * 262144 + g * 32768 + (h >> 1) * 16384 + ((h & 1) * 4 + q) * 2048 + r * 64 + (n >> 2) * 16, v);
            if (lane == 0) st4(WS_EL + layer * 4096 + g * 512 + r * 16 + h * 4, nn(f2u(M + __builtin_amdgcn_logf(Ls))));
            STAMP(7 + layer * 12);
            if (layer == 0) {
#pragma unroll
                for (int i = 0; i < 4; ++i) { const int row = ((w - 4) * 4 + i) * 16 + (lane >> 2);
                    *(LAS v4u*)(L + L_SO + row * 64 + (((lane & 3) ^ (((row >> 3) & 1) << 1)) * 16)) = kst[i]; }
                for (int it = 0;; ++it) {
                    CFENCE();
                    const v4u t0 = ld16(WS_TAB + (w - 4) * 2048 + lane * 16), t1 = ld16(WS_TAB + (w - 4) * 2048 + 1024 + lane * 16);
                    *(LAS v4u*)(L + L_TAB + (w - 4) * 2048 + lane * 16) = t0; *(LAS v4u*)(L + L_TAB + (w - 4) * 2048 + 1024 + lane * 16) = t1;
                    if (__all(vok(t0) && vok(t1))) break;
                    if ((it & 7) == 7) bump();
                    if (it > SPIN_CAP) break;
                }
                CFENCE();
            }
        }
        {
            v4u of[2][2];
            unsigned le[8];
            if (w < 4) {
                const int o0 = WS_EC + layer * 262144 + g * 32768 + hp * 16384 + w * 4096 + n * 64 + q * 16;
                const int l0 = WS_EL + layer * 4096 + g * 512 + q * 64 + (hp * 2 + (w >> 1)) * 4;
                nap<D_EC>();
                for (int it = 0;; ++it) {
                    CFENCE();
#pragma unroll
                    for (int ks = 0; ks < 2; ++ks)
#pragma unroll
                        for (int st = 0; st < 2; ++st) of[ks][st] = ld16(o0 + ks * 2048 + st * 1024);
#pragma unroll
                    for (int j = 0; j < 8; ++j) le[j] = ld4(l0 + (j >> 2) * 256 + (j & 3) * 16);
                    bool ok = vok(of[0][0]) && vok(of[0][1]) && vok(of[1][0]) && vok(of[1][1]);
#pragma unroll
                    for (int j = 0; j < 8; ++j) ok = ok && le[j] != INV;
                    if (__all(ok)) break;
                    if (layer == 0 && (it & 7) == 7) bump();
                    if (it > SPIN_CAP) break;
                }
                CFENCE();
            }
            STAMP(8 + layer * 12);
            if (layer == 0) BAR();
            if (w < 4) {
                if (layer == 0) {
                    const int aa = n >> 2, p = n & 3;
#pragma unroll
                    for (int ks = 0; ks < 2; ++ks)
#pragma unroll
                        for (int ct = 0; ct < 2; ++ct) {
                            v2u t[2];
#pragma unroll
                            for (int tt = 0; tt < 2; ++tt) { const int row = (w * 2 + ks) * 32 + q * 8 + tt * 4 + aa;
                                t[tt] = trd(L + L_SO + row * 64 + (((2 * ct + (p >> 1)) ^ (((row >> 3) & 1) << 1)) * 16) + 8 * (p & 1)); }
                            wof[ks][ct] = v4u{t[0].x, t[0].y, t[1].x, t[1].y};
                        }
                }
                f4 acc[2][2];
#pragma unroll
                for (int st = 0; st < 2; ++st)
#pragma unroll
                    for (int ct = 0; ct < 2; ++ct) acc[st][ct] = mfma32(of[1][st], wof[1][ct], mfma32(of[0][st], wof[0][ct], f4{0, 0, 0, 0}));
                float M = u2f(le[0]);
#pragma unroll
                for (int j = 1; j < 8; ++j) M = fmaxf(M, u2f(le[j]));
                M = max32(max16(M));
                float e[8], Ls = 0.f;
#pragma unroll
                for (int j = 0; j < 8; ++j) { e[j] = __builtin_amdgcn_exp2f(u2f(le[j]) - M); Ls += e[j]; }
                Ls = red32(Ls, Ls); Ls = red16(Ls, Ls);
                float pc[2];
#pragma unroll
                for (int ct = 0; ct < 2; ++ct) {
                    float s = 0.f;
#pragma unroll
                    for (int st = 0; st < 2; ++st) s += e[st * 4] * acc[st][ct].x + e[st * 4 + 1] * acc[st][ct].y + e[st * 4 + 2] * acc[st][ct].z + e[st * 4 + 3] * acc[st][ct].w;
                    pc[ct] = s;
                }
                float s = red32(pc[0], pc[1]);
                s = red16(s, s) * __builtin_amdgcn_rcpf(Ls);
                if ((q & 1) == 0) *(LAS float*)(L + L_WR + w * 128 + (q >> 1) * 64 + n * 4) = s;
            }
            BAR();
            if (w == 4 && lane < 16) {
                float s0 = 0.f, s1 = 0.f;
#pragma unroll
                for (int ww = 0; ww < 4; ++ww) { const f2 v = *(LAS f2*)(L + L_WR + ww * 128 + lane * 8); s0 += v.x; s1 += v.y; }
                const unsigned wd = nn(pk(s0, s1));
                const v4u v = {wd, dppu<0x55>(wd), dppu<0xAA>(wd), dppu<0xFF>(wd)};
                if ((lane & 3) == 0) st16(WS_EA + layer * 16384 + (g * 2 + hp) * 1024 + cb * 64 + lane * 4, v);
            }
            STAMP(9 + layer * 12);
        }
        if (layer == 1 && b != 0) return;
        {
            const int d = w * 64 + (lane & 7) * 8 + ((lane >> 5) & 1) * 4 + ((lane >> 4) & 1) * 2 + ((lane >> 3) & 1);
            const float xp = *(LAS float*)(L + L_XS + d * 4);
            const int o0 = WS_EA + layer * 16384 + (lane >> 3) * 1024 + w * 128 + (lane & 7) * 16;
            v4u e0, e1;
            nap<D_EA>();
            for (int it = 0;; ++it) {
                CFENCE();
                e0 = ld16(o0); e1 = ld16(o0 + 8192);
                if (__all(vok(e0) && vok(e1))) break;
                if (layer == 0 && (it & 7) == 7) bump();
                if (it > SPIN_CAP) break;
            }
            CFENCE();
            STAMP(10 + layer * 12);
            const float v0 = bflo(e0.x) + bflo(e1.x), v1 = bfhi(e0.x) + bfhi(e1.x), v2 = bflo(e0.y) + bflo(e1.y), v3 = bfhi(e0.y) + bfhi(e1.y);
            const float v4 = bflo(e0.z) + bflo(e1.z), v5 = bfhi(e0.z) + bfhi(e1.z), v6 = bflo(e0.w) + bflo(e1.w), v7 = bfhi(e0.w) + bfhi(e1.w);
            const float z0 = red16(red32(v0, v4), red32(v2, v6)), z1 = red16(red32(v1, v5), red32(v3, v7));
            const bool hi = (lane & 8) != 0;
            const float att = (hi ? z1 : z0) + dpp<ROR8>(hi ? z0 : z1);
            const float x1 = bfr(xp + bfr(att));
            {
                const float dx = x1 - mu;
                float s = red32(dx, dx * dx);
                s = rowsum(red16(s, s));
                if ((lane & 31) == 0) *(LAS float*)(L + L_ST1 + w * 8 + (lane >> 5) * 4) = s;
            }
            BAR();
            float S1 = 0.f, S2 = 0.f;
#pragma unroll
            for (int j = 0; j < 4; ++j) { const f4 v = *(LAS f4*)(L + L_ST1 + j * 16); S1 += v.x + v.z; S2 += v.y + v.w; }
            S1 *= (1.0f / 512.0f);
            const float mu1 = mu + S1;
            const float var = fmaxf(fmaf(S2, 1.0f / 512.0f, -S1 * S1), 0.f);
            const float r1 = __builtin_amdgcn_rsqf(var + 1e-5f);
            const float r2 = __builtin_amdgcn_rsqf(fmaf(var * r1, r1, 1e-5f));
            const unsigned hb = pk((x1 - mu1) * (r1 * r2), 0.f) & 0xffffu;
            const unsigned ex = ((hb >> 7) & 0xffu) - TE0;
            const bool inr = ex < 16u;
            const unsigned idx = inr ? ((ex << 8) | ((hb >> 15) << 7) | (hb & 127u)) : 0u;
            float y = bflo(*(LAS u16*)(L + L_TAB + idx * 2));
            unsigned long long miss = __ballot(!inr);
            while (miss) {
                const int src = __builtin_ctzll(miss);
                miss &= miss - 1;
                const float h = u2f((unsigned)__builtin_amdgcn_readlane((int)hb, src) << 16);
                float acc = 0.f;
                for (int f = lane * 2; f < NF; f += 128) {
                    const unsigned a1 = *(const unsigned*)(a.w1 + f), a2 = *(const unsigned*)(a.b1 + f), a3 = *(const unsigned*)(a.w2 + f);
                    acc = mix2(h, bflo(a1), bfhi(a1), bflo(a2), bfhi(a2), a3, acc);
                }
                acc = red32(acc, acc); acc = rowsum(red16(acc, acc));
                if (lane == src) y = bfr(bfr(acc) + b2v);
            }
            const float x2 = bfr(x1 + y);
            if (layer == 1) {
                a.out[d] = (u16)(f2u(x2) >> 16);
                STAMP(11 + layer * 12);
                return;
            }
            *(LAS float*)(L + L_XS + d * 4) = x2;
            *(LAS u16*)(L + L_XB + d * 2) = (u16)(f2u(x2) >> 16);
            {
                const float dx = x2 - mu1;
                float s = red32(dx, dx * dx);
                s = rowsum(red16(s, s));
                if ((lane & 31) == 0) *(LAS float*)(L + L_ST2 + w * 8 + (lane >> 5) * 4) = s;
            }
            BAR();
            f4 c = {0, 0, 0, 0};
            c = mfma32(wqf[0], *(LAS v4u*)(L + L_XB + kb * 128 + q * 16), c);
            c = mfma32(wqf[1], *(LAS v4u*)(L + L_XB + kb * 128 + 64 + q * 16), c);
            if (n == 0) st16(WS_EB + 131072 + g * 16384 + kb * 2048 + hq * 512 + w * 64 + q * 16, v4u{nn(f2u(c.x)), nn(f2u(c.y)), nn(f2u(c.z)), nn(f2u(c.w))});
            STAMP(11 + layer * 12);
            float T1 = 0.f, T2 = 0.f;
#pragma unroll
            for (int j = 0; j < 4; ++j) { const f4 v = *(LAS f4*)(L + L_ST2 + j * 16); T1 += v.x + v.z; T2 += v.y + v.w; }
            T1 *= (1.0f / 512.0f);
            mu = mu1 + T1;
            rsig = __builtin_amdgcn_rsqf(fmaxf(fmaf(T2, 1.0f / 512.0f, -T1 * T1), 0.f) + 1e-5f);
        }
    }
}

}

extern "C" void kernel_launch(void* const* d_in, const int* in_sizes, int n_in,
                              void* d_out, int out_size, void* d_ws, size_t ws_size,
                              hipStream_t stream) {
    fz::Args a;
    a.x = (const u16*)d_in[0]; a.kc = (const u16*)d_in[1]; a.vc = (const u16*)d_in[2];
    a.wq = (const u16*)d_in[3]; a.wo = (const u16*)d_in[4];
    a.w1 = (const u16*)d_in[6]; a.b1 = (const u16*)d_in[7]; a.w2 = (const u16*)d_in[8]; a.b2 = (const u16*)d_in[9];
    a.out = (u16*)d_out; a.ws = (unsigned char*)d_ws;
    hipFuncSetAttribute((const void*)fz::fused, hipFuncAttributeMaxDynamicSharedMemorySize, fz::LDS_BYTES);
    fz::fused<<<256, 512, fz::LDS_BYTES, stream>>>(a);
}
```

```cpp
#include <hip/hip_runtime.h>
#include <hip/hip_bf16.h>
#include <stdint.h>

typedef unsigned short u16;
typedef unsigned v4u __attribute__((ext_vector_type(4)));
typedef unsigned v2u __attribute__((ext_vector_type(2)));
typedef float f4 __attribute__((ext_vector_type(4)));
typedef float f2 __attribute__((ext_vector_type(2)));
typedef short s4 __attribute__((ext_vector_type(4)));
typedef __bf16 b8 __attribute__((ext_vector_type(8)));
typedef __bf16 b2 __attribute__((ext_vector_type(2)));
#define LAS __attribute__((address_space(3)))
#define GAS __attribute__((address_space(1)))

#define D_EB 8
#define D_EC 24
#define D_EA 20
#define CODE_PF 16384
#define TA 3
#define TB 12
#define D_K 8
#define D_EC0 4
#define TG0 2
#define TG 1
#define VH 4
#define WN 4
#define TB2 5
#define STAMPS 0

namespace fz {

constexpr int SK = 65536, WIN = 4096, NF = 1792;
constexpr unsigned INV = 0xFFFFFFFFu;
constexpr int WS_R = 0;
constexpr int WS_TAB = 8192;
constexpr int WS_EB = 16384;
constexpr int WS_EC = WS_EB + 3 * 131072;
constexpr int WS_EL = WS_EC + 2 * 262144;
constexpr int WS_EA = WS_EL + 2 * 65536;
constexpr int WS_END = WS_EA + 2 * 16384;
constexpr int WS_STAMP = 2 << 20;
constexpr int L_STG = 0;
constexpr int L_SQ = 32768;
constexpr int L_SO = L_SQ + 16384;
constexpr int L_TAB = L_SO + 16384;
constexpr int L_OW = L_TAB + 8192;
constexpr int L_QS = L_OW + 4 * 4 * 132 * 4;
constexpr int L_XB = L_QS + 1024;
constexpr int L_XS = L_XB + 1024;
constexpr int L_M = L_XS + 2048;
constexpr int L_WR = L_M + 128;
constexpr int L_ST1 = L_WR + 512;
constexpr int L_ST2 = L_ST1 + 64;
constexpr int L_TR = L_ST2 + 64;
constexpr int L_GATE = L_TR + 512;
constexpr int L_END = L_GATE + 16;
constexpr int LDS_BYTES = 100 * 1024;
static_assert(L_END <= LDS_BYTES, "lds");
constexpr int TE0 = 115;
constexpr int SPIN_CAP = 1 << 22;
constexpr unsigned long long MARGIN = 20;

__device__ __forceinline__ float u2f(unsigned u) { return __builtin_bit_cast(float, u); }
__device__ __forceinline__ unsigned f2u(float f) { return __builtin_bit_cast(unsigned, f); }
__device__ __forceinline__ float bflo(unsigned u) { return u2f(u << 16); }
__device__ __forceinline__ float bfhi(unsigned u) { return u2f(u & 0xffff0000u); }
__device__ __forceinline__ float bfr(float x) { return (float)(__bf16)x; }
__device__ __forceinline__ unsigned pk(float lo, float hi) { b2 v = {(__bf16)lo, (__bf16)hi}; return __builtin_bit_cast(unsigned, v); }
__device__ __forceinline__ unsigned nn(unsigned w) { return w == INV ? 0x7fc07fc0u : w; }
__device__ __forceinline__ f4 mfma32(v4u a, v4u b, f4 c) { return __builtin_amdgcn_mfma_f32_16x16x32_bf16(__builtin_bit_cast(b8, a), __builtin_bit_cast(b8, b), c, 0, 0, 0); }
__device__ __forceinline__ f4 mfma16(v2u a, v2u b, f4 c) { return __builtin_amdgcn_mfma_f32_16x16x16bf16_1k(__builtin_bit_cast(s4, a), __builtin_bit_cast(s4, b), c, 0, 0, 0); }
__device__ __forceinline__ v2u trd(LAS unsigned char* p) { return __builtin_bit_cast(v2u, __builtin_amdgcn_ds_read_tr16_b64_v4i16((LAS s4*)p)); }
template <int CTRL> __device__ __forceinline__ float dpp(float x) { return u2f(__builtin_amdgcn_mov_dpp(f2u(x), CTRL, 0xf, 0xf, true)); }
template <int CTRL> __device__ __forceinline__ unsigned dppu(unsigned x) { return (unsigned)__builtin_amdgcn_mov_dpp((int)x, CTRL, 0xf, 0xf, true); }
constexpr int ROR8 = 0x128, ROR4 = 0x124, ROR2 = 0x122, ROR1 = 0x121;
__device__ __forceinline__ float red32(float a, float b) { auto r = __builtin_amdgcn_permlane32_swap(f2u(a), f2u(b), false, false); return u2f(r[0]) + u2f(r[1]); }
__device__ __forceinline__ float red16(float a, float b) { auto r = __builtin_amdgcn_permlane16_swap(f2u(a), f2u(b), false, false); return u2f(r[0]) + u2f(r[1]); }
__device__ __forceinline__ float mx2(float a, float b) { return fmaxf(a, b); }
__device__ __forceinline__ float mx3(float a, float b, float c) { return fmaxf(fmaxf(a, b), c); }
__device__ __forceinline__ float max32(float a) { auto r = __builtin_amdgcn_permlane32_swap(f2u(a), f2u(a), false, false); return mx2(u2f(r[0]), u2f(r[1])); }
__device__ __forceinline__ float max16(float a) { auto r = __builtin_amdgcn_permlane16_swap(f2u(a), f2u(a), false, false); return mx2(u2f(r[0]), u2f(r[1])); }
__device__ __forceinline__ float dsel(unsigned p, unsigned sel, float acc) { return __builtin_amdgcn_fdot2_f32_bf16(__builtin_bit_cast(b2, p), __builtin_bit_cast(b2, sel), acc, false); }
__device__ __forceinline__ f2 mixp(f2 h, float w1, float b1, float w2, f2 acc) {
    const float c0 = -2.0f * 0.7978845608028654f * 1.4426950408889634f, c1 = c0 * 0.044715f;
    const f2 u = h * w1 + b1;
    const f2 z = u * (u * u * c1 + c0);
    const f2 d = f2{__builtin_amdgcn_exp2f(z.x), __builtin_amdgcn_exp2f(z.y)} + 1.0f;
    const f2 gq = u * f2{__builtin_amdgcn_rcpf(d.x), __builtin_amdgcn_rcpf(d.y)};
    return gq * w2 + acc;
}
__device__ __forceinline__ bool anynan(float x) { return __any(x != x); }
#define PIN(x) asm volatile("" : "+v"(x))
__device__ __forceinline__ float rowsum(float x) { x += dpp<ROR8>(x); x += dpp<ROR4>(x); x += dpp<ROR2>(x); x += dpp<ROR1>(x); return x; }
#define BAR() asm volatile("s_waitcnt lgkmcnt(0)\n\ts_barrier" ::: "memory")
#define CFENCE() asm volatile("" ::: "memory")
#define SB() __builtin_amdgcn_sched_barrier(0)
template <int N> __device__ __forceinline__ void nap() {
#pragma unroll
    for (int i = 0; i < N / 8; ++i) __builtin_amdgcn_s_sleep(8);
    if (N % 8) __builtin_amdgcn_s_sleep(N % 8);
}
__device__ __forceinline__ bool vok(v4u a) { return a.x != INV && a.y != INV && a.z != INV && a.w != INV; }

__device__ __forceinline__ float gelu(float u) {
    const float c0 = -2.0f * 0.7978845608028654f * 1.4426950408889634f, c1 = c0 * 0.044715f;
    float e = __builtin_amdgcn_exp2f(u * fmaf(u * u, c1, c0));
    return u * __builtin_amdgcn_rcpf(1.0f + e);
}
__device__ __forceinline__ float mix2(float h, float w1a, float w1b, float b1a, float b1b, unsigned w2p, float acc) {
    unsigned m = pk(h * w1a, h * w1b);
    unsigned u = pk(bflo(m) + b1a, bfhi(m) + b1b);
    unsigned gq = pk(gelu(bflo(u)), gelu(bfhi(u)));
    return __builtin_amdgcn_fdot2_f32_bf16(__builtin_bit_cast(b2, gq), __builtin_bit_cast(b2, w2p), acc, false);
}

struct Args {
    const u16 *x, *kc, *vc, *wq, *wo, *w1, *b1, *w2, *b2;
    u16* out;
    unsigned char* ws;
};

#define FST(i) do {} while (0)
#define FLUSH() do {} while (0)
#define STAMP(i) do {} while (0)

__global__ void __launch_bounds__(512, 2) fused(Args a) {
    extern __shared__ __attribute__((aligned(16))) unsigned char smem[];
    LAS unsigned char* L = (LAS unsigned char*)smem;
    const int b = blockIdx.x, tid = threadIdx.x;
    v4u cpf, cpf2;
    {
        unsigned long long pc;
        unsigned co, co2;
        asm volatile("s_getpc_b64 %4\n\tv_lshlrev_b32 %2, 4, %5\n\tv_min_u32 %3, %6, %2\n\tv_add_u32 %3, %7, %3\n\tglobal_load_dwordx4 %0, %2, %4\n\tglobal_load_dwordx4 %1, %3, %4"
                     : "=&v"(cpf), "=&v"(cpf2), "=&v"(co), "=&v"(co2), "=&s"(pc) : "v"(tid), "i"(CODE_PF - 8192 - 16), "i"(8192) : "memory");
        SB();
    }
    const int w = __builtin_amdgcn_readfirstlane(tid >> 6), lane = tid & 63;
    const int w3 = w & 3;
    const int g = b & 7, r = b >> 3;
    const int kb = r & 7, hq = r >> 3, hp = r >> 4, cb = r & 15;
    const int n = lane & 15, q = lane >> 4;
    const __amdgpu_buffer_rsrc_t rs = __builtin_amdgcn_make_buffer_rsrc((void*)a.ws, 0, WS_END, 0x00020000);
    auto ld16 = [&](int off) -> v4u { return __builtin_amdgcn_raw_buffer_load_b128(rs, off, 0, 16); };
    auto ld4 = [&](int off) -> unsigned { return __builtin_amdgcn_raw_buffer_load_b32(rs, off, 0, 16); };
    auto st16 = [&](int off, v4u v) { __builtin_amdgcn_raw_buffer_store_b128(v, rs, off, 0, 16); };
    auto st4 = [&](int off, unsigned v) { __builtin_amdgcn_raw_buffer_store_b32(v, rs, off, 0, 16); };
    const unsigned long long t_start = __builtin_amdgcn_s_memrealtime();
    auto bump = [&](bool tok) {
        const unsigned long long t = __builtin_amdgcn_s_memrealtime();
        if (lane == 0 || (lane == 1 && r == 0 && tok))
            __builtin_amdgcn_raw_buffer_store_b64(__builtin_bit_cast(v2u, t), rs, lane == 0 ? WS_R + g * 512 + r * 16 + 8 : WS_R + 4096 + g * 128, 0, 16);
    };
    auto within = [&](unsigned lo, unsigned hi, unsigned long long lim) -> bool { return (((unsigned long long)hi << 32) | lo) - t_start <= lim - t_start; };
    volatile LAS unsigned* gate = (volatile LAS unsigned*)(L + L_GATE);
    int vst = 0; (void)vst;
    STAMP(0);
    if (w == 0) *(LAS v4u*)(L + L_GATE) = v4u{0u, 0u, 0u, 0u};

    float mu, rsig;
    auto ln_stats = [&](f4 xa, f4 xb) {
        float s = ((xa.x + xa.y) + (xa.z + xa.w)) + ((xb.x + xb.y) + (xb.z + xb.w));
        s = red32(s, s); s = red16(s, s); s = rowsum(s);
        mu = s * (1.0f / 512.0f);
        const f4 da = xa - mu, db = xb - mu;
        float v = ((da.x * da.x + da.y * da.y) + (da.z * da.z + da.w * da.w)) + ((db.x * db.x + db.y * db.y) + (db.z * db.z + db.w * db.w));
        v = red32(v, v); v = red16(v, v); v = rowsum(v);
        rsig = __builtin_amdgcn_rsqf(v * (1.0f / 512.0f) + 1e-5f);
    };

    if (w < 4) {
        const size_t kvb = ((size_t)g * SK + (SK - WIN) + r * 128 + w * 4) * 128;
        const __amdgpu_buffer_rsrc_t rk = __builtin_amdgcn_make_buffer_rsrc((void*)(a.kc + kvb), 0, 32768 - 3072, 0x00020000);
        const __amdgpu_buffer_rsrc_t rv = __builtin_amdgcn_make_buffer_rsrc((void*)(a.vc + kvb), 0, 32768 - 3072, 0x00020000);
        const __amdgpu_buffer_rsrc_t ro = __builtin_amdgcn_make_buffer_rsrc((void*)(a.wo + ((size_t)(g * 2 + hp) * 256 + w * 64) * 512 + cb * 32), 0, 64 * 1024 - 960, 0x00020000);
        unsigned sel_lo = 0x00003f80u, sel_hi = 0x3f800000u;
        asm volatile("" : "+s"(sel_lo), "+s"(sel_hi));
        const unsigned mk0 = (n >> 2) == 0 ? ~0u : 0u, mk1 = (n >> 2) == 1 ? ~0u : 0u, mk2 = (n >> 2) == 2 ? ~0u : 0u, mk3 = (n >> 2) == 3 ? ~0u : 0u;
        LAS unsigned char* stg = L + L_STG + w * 8192;
        const float b2v = bflo(a.b2[0]);
        const v4u x0v = *(const v4u*)(a.x + lane * 8);
        BAR();
        nap<D_K>();
        v4u sb[20];
        auto issue = [&](int j) {
            if (j < 8) sb[j] = __builtin_amdgcn_raw_buffer_load_b128(rk, j * 4096 + lane * 16, 0, 2);
            else if (j < 16) sb[j] = __builtin_amdgcn_raw_buffer_load_b128(rv, (j - 8) * 4096 + lane * 16, 0, 2);
            else if (j < 20) sb[j] = __builtin_amdgcn_raw_buffer_load_b128(ro, ((j - 16) * 16 + (lane >> 2)) * 1024 + (lane & 3) * 16, 0, 2);
            SB();
        };
#pragma unroll
        for (int j = 0; j < WN; ++j) issue(j);
        {
            const f4 xa = {bflo(x0v.x), bfhi(x0v.x), bflo(x0v.y), bfhi(x0v.y)}, xb = {bflo(x0v.z), bfhi(x0v.z), bflo(x0v.w), bfhi(x0v.w)};
            ln_stats(xa, xb);
            if (w == 2) { *(LAS f4*)(L + L_XS + lane * 32) = xa; *(LAS f4*)(L + L_XS + lane * 32 + 16) = xb; }
        }
        asm volatile("" :: "v"(cpf), "v"(cpf2));
        STAMP(1);
#pragma unroll
        for (int i = 0; i < 8; ++i) {
            const int row = i * 4 + q; *(LAS v4u*)(stg + row * 256 + ((n ^ (row & 15)) * 16)) = sb[i];
            SB();
            issue(i + WN);
        }
        STAMP(3);
        v4u kf[2][4], vf[8], wof[2][2];
#pragma unroll
        for (int t = 0; t < 2; ++t)
#pragma unroll
            for (int ks = 0; ks < 4; ++ks) kf[t][ks] = *(LAS v4u*)(stg + (t * 16 + n) * 256 + (((ks * 4 + q) ^ n) * 16));
        SB();
#pragma unroll
        for (int i = 0; i < VH; ++i) {
            const int row = i * 4 + q; *(LAS v4u*)(stg + row * 256 + ((n ^ ((row & 7) << 1)) * 16)) = sb[8 + i];
            SB();
            issue(8 + i + WN);
        }

#pragma unroll
        for (int layer = 0; layer < 2; ++layer) {
            STAMP(4 + layer * 12);
            BAR();
            if (layer == 1) FST(32);
            {
                v4u qf[4];
#pragma unroll
                for (int ks = 0; ks < 4; ++ks) qf[ks] = *(LAS v4u*)(L + L_QS + (n & 3) * 256 + ks * 64 + q * 16);
                SB();
                f4 s0 = {0, 0, 0, 0}, s1 = {0, 0, 0, 0};
#pragma unroll
                for (int ks = 0; ks < 4; ++ks) { s0 = mfma32(kf[0][ks], qf[ks], s0); s1 = mfma32(kf[1][ks], qf[ks], s1); }
                const float C2 = 0.08838834764831845f * 1.4426950408889634f;
                s0 *= C2; s1 *= C2;
                float m = fmaxf(fmaxf(fmaxf(s0.x, s0.y), fmaxf(s0.z, s0.w)), fmaxf(fmaxf(s1.x, s1.y), fmaxf(s1.z, s1.w)));
                m = max32(max16(m));
                float e[8];
                e[0] = __builtin_amdgcn_exp2f(s0.x - m); e[1] = __builtin_amdgcn_exp2f(s0.y - m);
                e[2] = __builtin_amdgcn_exp2f(s0.z - m); e[3] = __builtin_amdgcn_exp2f(s0.w - m);
                e[4] = __builtin_amdgcn_exp2f(s1.x - m); e[5] = __builtin_amdgcn_exp2f(s1.y - m);
                e[6] = __builtin_amdgcn_exp2f(s1.z - m); e[7] = __builtin_amdgcn_exp2f(s1.w - m);
                const v4u pf = {pk(e[0], e[1]), pk(e[2], e[3]), pk(e[4], e[5]), pk(e[6], e[7])};
                STAMP(5 + layer * 12);
                if (layer == 0) {
#pragma unroll
                    for (int i = VH; i < 8; ++i) {
                        const int row = i * 4 + q; *(LAS v4u*)(stg + row * 256 + ((n ^ ((row & 7) << 1)) * 16)) = sb[8 + i];
                        SB();
                        issue(8 + i + WN);
                    }
                    const int row = q * 4 + (n >> 2), p = n & 3;
#pragma unroll
                    for (int c = 0; c < 8; ++c) {
                        LAS unsigned char* ap = stg + row * 256 + (((2 * c + (p >> 1)) ^ ((row & 7) << 1)) * 16) + 8 * (p & 1);
                        const v2u t0 = trd(ap), t1 = trd(ap + 4096);
                        vf[c] = v4u{t0.x, t0.y, t1.x, t1.y};
                    }
                }
                STAMP(6 + layer * 12);
                f4 oa = {0, 0, 0, 0}, ob = {0, 0, 0, 0};
                { const v4u pm = pf & mk0; oa = mfma32(vf[0], pm, oa); ob = mfma32(vf[4], pm, ob); }
                { const v4u pm = pf & mk1; oa = mfma32(vf[1], pm, oa); ob = mfma32(vf[5], pm, ob); }
                { const v4u pm = pf & mk2; oa = mfma32(vf[2], pm, oa); ob = mfma32(vf[6], pm, ob); }
                { const v4u pm = pf & mk3; oa = mfma32(vf[3], pm, oa); ob = mfma32(vf[7], pm, ob); }
                float ls = ((e[0] + e[1]) + (e[2] + e[3])) + ((e[4] + e[5]) + (e[6] + e[7]));
                ls = red32(ls, ls); ls = red16(ls, ls);
                if (lane < 4) { *(LAS float*)(L + L_M + n * 32 + w * 4) = m; *(LAS float*)(L + L_M + n * 32 + 16 + w * 4) = ls; }
                *(LAS f4*)(L + L_OW + (w * 4 + (n & 3)) * 528 + (n >> 2) * 64 + q * 16) = oa;
                *(LAS f4*)(L + L_OW + (w * 4 + (n & 3)) * 528 + (n >> 2) * 64 + 256 + q * 16) = ob;
            }
            if (layer == 1) FST(35);
            BAR();
            if (layer == 1) FST(36);
            {
                int o0 = WS_EC + layer * 262144 + g * 32768 + hp * 16384 + w * 4096 + n * 64 + q * 16;
                int l0 = WS_EL + layer * 65536 + g * 8192 + (hp * 2 + (w >> 1)) * 2048 + q * 256;
                int wa = L_WR + w * 128 + (q >> 1) * 64 + n * 4;
                PIN(o0); PIN(l0); PIN(wa);
                if (layer == 0) {
#pragma unroll
                    for (int i = 0; i < 4; ++i) { const int row = i * 16 + (lane >> 2);
                        *(LAS v4u*)(stg + row * 64 + (((lane & 3) ^ (((row >> 3) & 1) << 1)) * 16)) = sb[16 + i]; }
                    const int aa = n >> 2, p = n & 3;
#pragma unroll
                    for (int ks = 0; ks < 2; ++ks)
#pragma unroll
                        for (int ct = 0; ct < 2; ++ct) {
                            v2u t[2];
#pragma unroll
                            for (int tt = 0; tt < 2; ++tt) { const int row = ks * 32 + q * 8 + tt * 4 + aa;
                                t[tt] = trd(stg + row * 64 + (((2 * ct + (p >> 1)) ^ (((row >> 3) & 1) << 1)) * 16) + 8 * (p & 1)); }
                            wof[ks][ct] = v4u{t[0].x, t[0].y, t[1].x, t[1].y};
                        }
                    nap<D_EC0>();
                } else nap<D_EC>();
                float s;
                for (int it = 0;; ++it) {
                    if (layer == 0 || it > 0) {
                        for (int i2 = 0;; ++i2) {
                            CFENCE();
                            const unsigned f0 = ld4(l0 - q * 256 + (lane & 31) * 64);
                            if (__all(f0 != INV)) break;
                            if (layer == 0 && (i2 & 7) == 7) bump(true);
                            if (i2 > SPIN_CAP) break;
                            nap<4>();
                        }
                    }
                    CFENCE();
                    v4u of[2][2];
                    unsigned le[8];
#pragma unroll
                    for (int j = 0; j < 8; ++j) le[j] = ld4(l0 + (j >> 2) * 1024 + (j & 3) * 64);
#pragma unroll
                    for (int ks = 0; ks < 2; ++ks)
#pragma unroll
                        for (int st = 0; st < 2; ++st) of[ks][st] = ld16(o0 + ks * 2048 + st * 1024);
                    STAMP(8 + layer * 12);
                    float M = mx3(mx3(u2f(le[0]), u2f(le[1]), u2f(le[2])), mx3(u2f(le[3]), u2f(le[4]), u2f(le[5])), mx2(u2f(le[6]), u2f(le[7])));
                    M = max32(max16(M));
                    float e[8];
#pragma unroll
                    for (int j = 0; j < 8; ++j) e[j] = __builtin_amdgcn_exp2f(u2f(le[j]) - M);
                    float Ls = ((e[0] + e[1]) + (e[2] + e[3])) + ((e[4] + e[5]) + (e[6] + e[7]));
                    Ls = red32(Ls, Ls); Ls = red16(Ls, Ls);
                    f4 acc[2][2];
#pragma unroll
                    for (int st = 0; st < 2; ++st)
#pragma unroll
                        for (int ct = 0; ct < 2; ++ct) acc[st][ct] = mfma32(of[1][st], wof[1][ct], mfma32(of[0][st], wof[0][ct], f4{0, 0, 0, 0}));
                    float pc[2];
#pragma unroll
                    for (int ct = 0; ct < 2; ++ct)
                        pc[ct] = ((e[0] * acc[0][ct].x + e[1] * acc[0][ct].y) + (e[2] * acc[0][ct].z + e[3] * acc[0][ct].w)) +
                                 ((e[4] * acc[1][ct].x + e[5] * acc[1][ct].y) + (e[6] * acc[1][ct].z + e[7] * acc[1][ct].w));
                    s = red32(pc[0], pc[1]);
                    s = red16(s, s) * __builtin_amdgcn_rcpf(Ls);
                    if (!anynan(s)) break;
                    bool ok = vok(of[0][0]) && vok(of[0][1]) && vok(of[1][0]) && vok(of[1][1]);
#pragma unroll
                    for (int j = 0; j < 8; ++j) ok = ok && le[j] != INV;
                    if (__all(ok)) break;
                    if (layer == 0 && (it & 7) == 7) bump(true);
                    if (it > SPIN_CAP) break;
                }
                CFENCE();
                if (layer == 1) FST(39);
                if ((q & 1) == 0) *(LAS float*)(L + wa) = s;
            }
            BAR();
            if (layer == 1) FST(40);
            STAMP(9 + layer * 12);
            if (layer == 1 && b != 0) { FLUSH(); return; }
            {
                const int d0 = w * 128 + n * 8 + (q >> 1) * 4 + (q & 1) * 2;
                float x1a, x1b;
                const f2 xp = *(LAS f2*)(L + L_XS + d0 * 4);
                int o0 = WS_EA + layer * 16384 + q * 1024 + w * 256 + n * 16;
                PIN(o0);
                nap<D_EA>();
                for (int it = 0;; ++it) {
                    CFENCE();
                    v4u e[4];
#pragma unroll
                    for (int i = 0; i < 4; ++i) e[i] = ld16(o0 + i * 4096);
                    STAMP(10 + layer * 12);
                    float v[8];
#pragma unroll
                    for (int j = 0; j < 4; ++j) {
                        v[2 * j] = dsel(e[3][j], sel_lo, dsel(e[2][j], sel_lo, dsel(e[1][j], sel_lo, bflo(e[0][j]))));
                        v[2 * j + 1] = dsel(e[3][j], sel_hi, dsel(e[2][j], sel_hi, dsel(e[1][j], sel_hi, bfhi(e[0][j]))));
                    }
                    const float z0 = red16(red32(v[0], v[4]), red32(v[2], v[6])), z1 = red16(red32(v[1], v[5]), red32(v[3], v[7]));
                    const unsigned at = pk(z0, z1);
                    const unsigned x1p = pk(xp.x + bflo(at), xp.y + bfhi(at));
                    x1a = bflo(x1p); x1b = bfhi(x1p);
                    if (!anynan(x1a + x1b)) break;
                    if (__all(vok(e[0]) && vok(e[1]) && vok(e[2]) && vok(e[3]))) break;
                    if (layer == 0 && (it & 7) == 7) bump(true);
                    if (it > SPIN_CAP) break;
                }
                CFENCE();
                {
                    const float da = x1a - mu, db = x1b - mu;
                    float s = red32(da + db, da * da + db * db);
                    s = rowsum(red16(s, s));
                    if ((lane & 31) == 0) *(LAS float*)(L + L_ST1 + w * 8 + (lane >> 5) * 4) = s;
                }
                if (layer == 0) FST(42);
                BAR();
                if (layer == 0) FST(43);
                const f4 va = *(LAS f4*)(L + L_ST1), vb = *(LAS f4*)(L + L_ST1 + 16);
                const float S1 = ((va.x + va.z) + (vb.x + vb.z)) * (1.0f / 512.0f), S2 = (va.y + va.w) + (vb.y + vb.w);
                const float mu1 = mu + S1;
                const float var = mx2(fmaf(S2, 1.0f / 512.0f, -S1 * S1), 0.f);
                const float r1 = __builtin_amdgcn_rsqf(var + 1e-5f);
                const float rr = r1 * __builtin_amdgcn_rsqf(fmaf(var * r1, r1, 1e-5f));
                const unsigned hb = pk((x1a - mu1) * rr, (x1b - mu1) * rr);
                const unsigned ta = (hb & 0x7fffu) - (TE0 << 7), tb = ((hb >> 16) & 0x7fffu) - (TE0 << 7);
                const bool ina = ta < 2048u, inb = tb < 2048u;
                const unsigned ia = (ta & 2047u) | ((hb >> 4) & 2048u), ib = (tb & 2047u) | ((hb >> 20) & 2048u);
                float ya = bflo(*(LAS u16*)(L + L_TAB + ia * 2)), yb = bflo(*(LAS u16*)(L + L_TAB + ib * 2));
                if (__any(!(ina && inb))) {
#pragma nounroll
                    for (int half = 0; half < 2; ++half) {
                        unsigned long long miss = __ballot(half ? !inb : !ina);
                        while (miss) {
                            const int src = __builtin_ctzll(miss);
                            miss &= miss - 1;
                            const unsigned hs = (unsigned)__builtin_amdgcn_readlane((int)hb, src);
                            const float h = half ? bfhi(hs) : bflo(hs);
                            f2 ac2 = {0.f, 0.f};
#pragma nounroll
                            for (int f = lane; f < NF; f += 64)
                                ac2 = mixp(f2{h, h}, bflo(*(LAS u16*)(L + L_SO + f * 2)), bflo(*(LAS u16*)(L + L_SO + 4096 + f * 2)), bflo(*(LAS u16*)(L + L_SO + 8192 + f * 2)), ac2);
                            float acc = ac2.x;
                            acc = red32(acc, acc); acc = rowsum(red16(acc, acc));
                            const float yy = bfr(bfr(acc) + b2v);
                            if (lane == src) { if (half) yb = yy; else ya = yy; }
                        }
                    }
                }
                const unsigned x2p = pk(x1a + ya, x1b + yb);
                if (layer == 1) {
                    *(unsigned*)(a.out + d0) = x2p;
                    STAMP(11 + layer * 12); FLUSH();
                    return;
                }
                *(LAS unsigned*)(L + L_XB + d0 * 2) = x2p;
                *(LAS f2*)(L + L_XS + d0 * 4) = f2{bflo(x2p), bfhi(x2p)};
                FST(45);
                BAR();
                FST(46);
                STAMP(11 + layer * 12);
                mu = mu1;
            }
        }
    } else {
        const __amdgpu_buffer_rsrc_t rq = __builtin_amdgcn_make_buffer_rsrc((void*)(a.wq + (size_t)kb * 64 * 4096 + (g * 4 + hq) * 128), 0, 64 * 8192 - 7936, 0x00020000);
        const int ro_ = lane < 32 ? WS_R + g * 512 + lane * 16 : WS_R + 4096 + (lane & 7) * 128;
        v4u kst[8];
#pragma unroll
        for (int i = 0; i < 4; ++i) kst[i] = __builtin_amdgcn_raw_buffer_load_b128(rq, (w3 * 16 + i * 4 + q) * 8192 + n * 16, 0, 2);
        v4u xf[2];
#pragma unroll
        for (int ks = 0; ks < 2; ++ks) xf[ks] = *(const v4u*)(a.x + kb * 64 + ks * 32 + q * 8);
        const v4u x0v = *(const v4u*)(a.x + lane * 8);
        if (w < 7) {
            const __amdgpu_buffer_rsrc_t rp = __builtin_amdgcn_make_buffer_rsrc((void*)(w == 4 ? a.w1 : w == 5 ? a.b1 : a.w2), 0, NF * 2, 0x00020000);
#pragma unroll
            for (int i = 0; i < 4; ++i) kst[4 + i] = __builtin_amdgcn_raw_buffer_load_b128(rp, i * 1024 + lane * 16, 0, 0);
        } else {
            const v4u iv = {INV, INV, INV, INV};
            st16(WS_EB + (lane >> 5) * 131072 + g * 16384 + kb * 2048 + hq * 512 + (lane & 31) * 16, iv);
            int o;
            if (lane < 32) o = WS_EB + 2 * 131072 + g * 16384 + kb * 2048 + hq * 512 + lane * 16;
            else if (lane < 40) o = WS_EA + ((lane >> 2) & 1) * 16384 + (g * 2 + hp) * 1024 + cb * 64 + (lane & 3) * 16;
            else if (lane < 48) o = WS_EL + ((lane >> 2) & 1) * 65536 + g * 8192 + (lane & 3) * 2048 + r * 64;
            else o = WS_TAB + b * 32 + (lane & 1) * 16;
            if (lane < 50) st16(o, iv);
            st16(WS_EC + g * 32768 + (lane >> 2) * 2048 + r * 64 + (lane & 3) * 16, iv);
            st16(WS_EC + 262144 + g * 32768 + (lane >> 2) * 2048 + r * 64 + (lane & 3) * 16, iv);
        }
        const float b2v = bflo(a.b2[0]);
        BAR();
#pragma unroll
        for (int i = 0; i < 4; ++i) { const int row = w3 * 16 + i * 4 + q;
            *(LAS v4u*)(L + L_SQ + row * 256 + ((n ^ (((row & 3) << 2) | ((row >> 2) & 3))) * 16)) = kst[i]; }
        if (w < 7) {
#pragma unroll
            for (int i = 0; i < 4; ++i) *(LAS v4u*)(L + L_SO + w3 * 4096 + i * 1024 + lane * 16) = kst[4 + i];
        } else {
            asm volatile("s_waitcnt vmcnt(0)" ::: "memory");
            const v2u t = __builtin_bit_cast(v2u, (unsigned long long)__builtin_amdgcn_s_memrealtime());
            if (lane == 0) st16(WS_R + g * 512 + r * 16, v4u{t.x, t.y, t.x, t.y});
        }
        if (n == 1) { xf[0] = v4u{0x3f803f80u, 0x3f803f80u, 0x3f803f80u, 0x3f803f80u}; xf[1] = xf[0]; }
        asm volatile("" :: "v"(cpf), "v"(cpf2));
        STAMP(1);
        asm volatile("s_waitcnt lgkmcnt(0)" ::: "memory");
        if (lane == 0) __hip_atomic_fetch_add((LAS unsigned*)(L + L_GATE), 1u, __ATOMIC_RELAXED, __HIP_MEMORY_SCOPE_WORKGROUP);
        while (*gate < 4u) __builtin_amdgcn_s_sleep(1);
        CFENCE();
        v4u wqf[2][2];
        {
            const int aa = n >> 2, p = n & 3;
#pragma unroll
            for (int ct = 0; ct < 2; ++ct)
#pragma unroll
                for (int ks = 0; ks < 2; ++ks) {
                    v2u t[2];
#pragma unroll
                    for (int tt = 0; tt < 2; ++tt) { const int row = ks * 32 + q * 8 + tt * 4 + aa, ch = 4 * w3 + 2 * ct + (p >> 1);
                        t[tt] = trd(L + L_SQ + row * 256 + ((ch ^ (((row & 3) << 2) | ((row >> 2) & 3))) * 16) + 8 * (p & 1)); }
                    wqf[ct][ks] = v4u{t[0].x, t[0].y, t[1].x, t[1].y};
                }
#pragma unroll
            for (int ct = 0; ct < 2; ++ct) {
                const f4 c = mfma32(wqf[ct][1], xf[1], mfma32(wqf[ct][0], xf[0], f4{0, 0, 0, 0}));
                if (n < 2) st16(WS_EB + n * 2 * 131072 + g * 16384 + kb * 2048 + hq * 512 + w3 * 128 + ct * 64 + q * 16, __builtin_bit_cast(v4u, c));
            }
        }
        SB();
        STAMP(2);
        f2 csum = {0.f, 0.f};
        auto fetch_q = [&](int layer, auto&& meanwhile) {
            int o0 = WS_EB + layer * 131072 + g * 16384 + (lane >> 5) * 2048 + w3 * 512 + (lane & 31) * 16;
            int qa = L_QS + (w3 * 128 + (lane & 31) * 4 + (lane >> 5) * 2) * 2;
            PIN(o0); PIN(qa);
            f2 qv;
            if (layer == 1) nap<D_EB>();
            bool ready = layer == 1;
            for (int it = 0;; ++it) {
                CFENCE();
                v4u e[4], c[4], rec = {0, 0, 0, 0};
                unsigned long long lim = 0;
                if (layer == 0) { lim = __builtin_amdgcn_s_memrealtime() - MARGIN; SB(); if (!ready && lane < 32) rec = ld16(ro_); }
#pragma unroll
                for (int i = 0; i < 4; ++i) e[i] = ld16(o0 + i * 4096);
                if (layer == 0) {
#pragma unroll
                    for (int i = 0; i < 4; ++i) c[i] = ld16(o0 + 2 * 131072 + i * 4096);
                    SB();
                    if (it == 0) meanwhile();
                    SB();
                    const f4 cs = (__builtin_bit_cast(f4, c[0]) + __builtin_bit_cast(f4, c[1])) + (__builtin_bit_cast(f4, c[2]) + __builtin_bit_cast(f4, c[3]));
                    csum = f2{red32(cs.x, cs.z), red32(cs.y, cs.w)};
                    if (!ready) ready = __all(lane >= 32 || within(rec.x, rec.y, lim) || within(rec.z, rec.w, lim));
                }
                const f4 es = (__builtin_bit_cast(f4, e[0]) + __builtin_bit_cast(f4, e[1])) + (__builtin_bit_cast(f4, e[2]) + __builtin_bit_cast(f4, e[3]));
                qv = (f2{red32(es.x, es.z), red32(es.y, es.w)} - csum * mu) * rsig;
                if (ready) {
                    if (!anynan(qv.x + qv.y)) break;
                    bool ok = vok(e[0]) && vok(e[1]) && vok(e[2]) && vok(e[3]);
                    if (layer == 0) ok = ok && vok(c[0]) && vok(c[1]) && vok(c[2]) && vok(c[3]);
                    if (__all(ok)) break;
                }
                if (layer == 0 && (it & 7) == 7) bump(false);
                if (it > SPIN_CAP) break;
            }
            CFENCE();
            *(LAS unsigned*)(L + qa) = pk(qv.x, qv.y);
        };
        float w1f[7], b1f[7], w2f[7];
            f2 tacc[8];
            const unsigned hbits0 = (unsigned)((b >> 7) << 15) | (unsigned)((TE0 << 7) + (b & 127) * 16);
            auto tab_pair = [&](int e) {
                const f2 h = {u2f((hbits0 + e) << 16), u2f((hbits0 + e + 8) << 16)};
                f2 acc = {0.f, 0.f};
#pragma unroll
                for (int j = 0; j < 7; ++j) acc = mixp(h, w1f[j], b1f[j], w2f[j], acc);
                asm volatile("" : "+v"(acc));
                tacc[e] = acc;
            };
#pragma unroll
            for (int j = 0; j < 7; ++j) { const int f = ((tid & 255) + 256 * j) * 2;
                w1f[j] = bflo(*(LAS u16*)(L + L_SO + f)); b1f[j] = bflo(*(LAS u16*)(L + L_SO + 4096 + f)); w2f[j] = bflo(*(LAS u16*)(L + L_SO + 8192 + f)); }
            ln_stats(f4{bflo(x0v.x), bfhi(x0v.x), bflo(x0v.y), bfhi(x0v.y)}, f4{bflo(x0v.z), bfhi(x0v.z), bflo(x0v.w), bfhi(x0v.w)});
            SB();
#pragma unroll
            for (int e = 0; e < TG; ++e) tab_pair(e);
            SB();
            STAMP(3);
            fetch_q(0, [&]() {
#pragma unroll
                for (int e = TG; e < TA; ++e) tab_pair(e);
            });
            if (w == 7) bump(true);
            STAMP(4);
            BAR();
#pragma unroll
            for (int e = TA; e < TB2; ++e) tab_pair(e);
            STAMP(5);
#pragma unroll
        for (int layer = 0; layer < 2; ++layer) {
            if (layer == 1) BAR();
            int ea = WS_EC + layer * 262144 + g * 32768 + (w3 >> 1) * 16384 + ((w3 & 1) * 4 + q) * 2048 + r * 64 + (n >> 2) * 16;
            int oa = L_OW + w3 * 528 + lane * 8;
            PIN(ea); PIN(oa);
            BAR();
            if (layer == 1) FST(36);
            {
                const f4 ma = *(LAS f4*)(L + L_M + w3 * 32), la = *(LAS f4*)(L + L_M + w3 * 32 + 16);
                f2 v[4];
#pragma unroll
                for (int ww = 0; ww < 4; ++ww) v[ww] = *(LAS f2*)(L + oa + ww * 2112);
                const float M = mx2(mx3(ma.x, ma.y, ma.z), ma.w);
                const float e0 = __builtin_amdgcn_exp2f(ma.x - M), e1 = __builtin_amdgcn_exp2f(ma.y - M), e2 = __builtin_amdgcn_exp2f(ma.z - M), e3 = __builtin_amdgcn_exp2f(ma.w - M);
                const float Ls = (e0 * la.x + e1 * la.y) + (e2 * la.z + e3 * la.w);
                const f2 oo = (v[0] * e0 + v[1] * e1) + (v[2] * e2 + v[3] * e3);
                const float inv = __builtin_amdgcn_rcpf(Ls);
                const unsigned wd = nn(pk(oo.x * inv, oo.y * inv));
                const v4u vv = {wd, dppu<0x55>(wd), dppu<0xAA>(wd), dppu<0xFF>(wd)};
                if ((lane & 3) == 0) st16(ea, vv);
                if (lane == 0) st4(WS_EL + layer * 65536 + g * 8192 + w3 * 2048 + r * 64, f2u(M + __builtin_amdgcn_logf(Ls)));
            }
            STAMP(7 + layer * 12);
            if (layer == 0) {
#pragma unroll
                for (int e = TB2; e < 8; ++e) tab_pair(e);
                float t8[8], t4[4];
#pragma unroll
                for (int j = 0; j < 8; ++j) t8[j] = red32(tacc[j].x, tacc[j].y);
#pragma unroll
                for (int j = 0; j < 4; ++j) t4[j] = rowsum(red16(t8[j], t8[j + 4]));
                if (n == 0) *(LAS f4*)(L + L_TR + w3 * 64 + q * 16) = f4{t4[0], t4[1], t4[2], t4[3]};
                STAMP(6);
                if (w == 7) {
                    for (int it = 0;; ++it) {
                        CFENCE();
                        v4u cur = {0, 0, 0, 0};
                        if (lane >= 32) cur = ld16(ro_);
                        if (__all(lane < 32 || within(cur.x, cur.y, __builtin_amdgcn_s_memrealtime()))) break;
                        if ((it & 3) == 3) bump(true);
                        if (it > SPIN_CAP) break;
                        nap<16>();
                    }
                    CFENCE();
                }
            }
            BAR();
            if (w == 4 && lane < 16) {
                f2 v[4];
#pragma unroll
                for (int ww = 0; ww < 4; ++ww) v[ww] = *(LAS f2*)(L + L_WR + ww * 128 + lane * 8);
                const f2 sv = (v[0] + v[1]) + (v[2] + v[3]);
                const unsigned wd = nn(pk(sv.x, sv.y));
                const v4u vv = {wd, dppu<0x55>(wd), dppu<0xAA>(wd), dppu<0xFF>(wd)};
                if ((lane & 3) == 0) st16(WS_EA + layer * 16384 + (g * 2 + hp) * 1024 + cb * 64 + lane * 4, vv);
            }
            if (layer == 0 && w == 5 && lane < 8) {
                f2 tv[4];
#pragma unroll
                for (int ww = 0; ww < 4; ++ww) tv[ww] = *(LAS f2*)(L + L_TR + ww * 64 + lane * 8);
                const f2 sv = (tv[0] + tv[1]) + (tv[2] + tv[3]);
                const unsigned wd = nn(pk(bfr(sv.x) + b2v, bfr(sv.y) + b2v));
                const v4u vv = {wd, dppu<0x55>(wd), dppu<0xAA>(wd), dppu<0xFF>(wd)};
                if ((lane & 3) == 0) st16(WS_TAB + b * 32 + lane * 4, vv);
            }
            STAMP(9 + layer * 12);
            if (layer == 1) { FLUSH(); return; }
            nap<D_EA>();
            for (int it = 0;; ++it) {
                CFENCE();
                const v4u t0 = ld16(WS_TAB + w3 * 2048 + lane * 16), t1 = ld16(WS_TAB + w3 * 2048 + 1024 + lane * 16);
                *(LAS v4u*)(L + L_TAB + w3 * 2048 + lane * 16) = t0; *(LAS v4u*)(L + L_TAB + w3 * 2048 + 1024 + lane * 16) = t1;
                if (__all(vok(t0) && vok(t1))) break;
                if ((it & 7) == 7) bump(true);
                if (it > SPIN_CAP) break;
            }
            CFENCE();
            int qo = WS_EB + 131072 + g * 16384 + kb * 2048 + hq * 512 + w3 * 128 + n * 64 + q * 16;
            int xa_ = L_XB + kb * 128 + q * 16;
            PIN(qo); PIN(xa_);
            BAR();
            BAR();
            {
                const v4u xa = *(LAS v4u*)(L + xa_), xb = *(LAS v4u*)(L + xa_ + 64);
                const f4 c0 = mfma32(wqf[0][1], xb, mfma32(wqf[0][0], xa, f4{0, 0, 0, 0})), c1 = mfma32(wqf[1][1], xb, mfma32(wqf[1][0], xa, f4{0, 0, 0, 0}));
                if (n < 2) st16(qo, __builtin_bit_cast(v4u, n == 0 ? c0 : c1));
            }
            STAMP(11 + layer * 12);
            SB();
            ln_stats(*(LAS f4*)(L + L_XS + lane * 32), *(LAS f4*)(L + L_XS + lane * 32 + 16));
            fetch_q(1, []() {});
            STAMP(16);
        }
    }
}

}

extern "C" void kernel_launch(void* const* d_in, const int* in_sizes, int n_in,
                              void* d_out, int out_size, void* d_ws, size_t ws_size,
                              hipStream_t stream) {
    fz::Args a;
    a.x = (const u16*)d_in[0]; a.kc = (const u16*)d_in[1]; a.vc = (const u16*)d_in[2];
    a.wq = (const u16*)d_in[3]; a.wo = (const u16*)d_in[4];
    a.w1 = (const u16*)d_in[6]; a.b1 = (const u16*)d_in[7]; a.w2 = (const u16*)d_in[8]; a.b2 = (const u16*)d_in[9];
    a.out = (u16*)d_out; a.ws = (unsigned char*)d_ws;
    (void)hipFuncSetAttribute((const void*)fz::fused, hipFuncAttributeMaxDynamicSharedMemorySize, fz::LDS_BYTES);
    fz::fused<<<256, 512, fz::LDS_BYTES, stream>>>(a);
}
```

```cpp
#define STAMPS 3
#include <hip/hip_runtime.h>
#include <stdio.h>
#include <hip/hip_bf16.h>
#include <stdint.h>

typedef unsigned short u16;
typedef unsigned v4u __attribute__((ext_vector_type(4)));
typedef unsigned v2u __attribute__((ext_vector_type(2)));
typedef float f4 __attribute__((ext_vector_type(4)));
typedef float f2 __attribute__((ext_vector_type(2)));
typedef short s4 __attribute__((ext_vector_type(4)));
typedef __bf16 b8 __attribute__((ext_vector_type(8)));
typedef __bf16 b2 __attribute__((ext_vector_type(2)));
#define LAS __attribute__((address_space(3)))
#define GAS __attribute__((address_space(1)))

#define D_EB 8
#define D_EC 24
#define D_EA 20
#define CODE_PF 16384
#define TA 3
#define TB 12
#define D_K 8
#define D_EC0 4
#define TG0 2
#define TG 1
#define VH 4
#define WN 4
#define TB2 5

namespace fz {

constexpr int SK = 65536, WIN = 4096, NF = 1792;
constexpr unsigned INV = 0xFFFFFFFFu;
constexpr int WS_R = 0;
constexpr int WS_TAB = 8192;
constexpr int WS_EB = 16384;
constexpr int WS_EC = WS_EB + 3 * 131072;
constexpr int WS_EL = WS_EC + 2 * 262144;
constexpr int WS_EA = WS_EL + 2 * 65536;
constexpr int WS_END = WS_EA + 2 * 16384;
constexpr int WS_STAMP = 2 << 20;
constexpr int L_STG = 0;
constexpr int L_SQ = 32768;
constexpr int L_SO = L_SQ + 16384;
constexpr int L_TAB = L_SO + 16384;
constexpr int L_OW = L_TAB + 8192;
constexpr int L_QS = L_OW + 4 * 4 * 132 * 4;
constexpr int L_XB = L_QS + 1024;
constexpr int L_XS = L_XB + 1024;
constexpr int L_M = L_XS + 2048;
constexpr int L_WR = L_M + 128;
constexpr int L_ST1 = L_WR + 512;
constexpr int L_ST2 = L_ST1 + 64;
constexpr int L_TR = L_ST2 + 64;
constexpr int L_GATE = L_TR + 512;
constexpr int L_END = L_GATE + 16;
constexpr int LDS_BYTES = 100 * 1024;
static_assert(L_END <= LDS_BYTES, "lds");
constexpr int TE0 = 115;
constexpr int SPIN_CAP = 1 << 22;
constexpr unsigned long long MARGIN = 20;

__device__ __forceinline__ float u2f(unsigned u) { return __builtin_bit_cast(float, u); }
__device__ __forceinline__ unsigned f2u(float f) { return __builtin_bit_cast(unsigned, f); }
__device__ __forceinline__ float bflo(unsigned u) { return u2f(u << 16); }
__device__ __forceinline__ float bfhi(unsigned u) { return u2f(u & 0xffff0000u); }
__device__ __forceinline__ float bfr(float x) { return (float)(__bf16)x; }
__device__ __forceinline__ unsigned pk(float lo, float hi) { b2 v = {(__bf16)lo, (__bf16)hi}; return __builtin_bit_cast(unsigned, v); }
__device__ __forceinline__ unsigned nn(unsigned w) { return w == INV ? 0x7fc07fc0u : w; }
__device__ __forceinline__ f4 mfma32(v4u a, v4u b, f4 c) { return __builtin_amdgcn_mfma_f32_16x16x32_bf16(__builtin_bit_cast(b8, a), __builtin_bit_cast(b8, b), c, 0, 0, 0); }
__device__ __forceinline__ f4 mfma16(v2u a, v2u b, f4 c) { return __builtin_amdgcn_mfma_f32_16x16x16bf16_1k(__builtin_bit_cast(s4, a), __builtin_bit_cast(s4, b), c, 0, 0, 0); }
__device__ __forceinline__ v2u trd(LAS unsigned char* p) { return __builtin_bit_cast(v2u, __builtin_amdgcn_ds_read_tr16_b64_v4i16((LAS s4*)p)); }
template <int CTRL> __device__ __forceinline__ float dpp(float x) { return u2f(__builtin_amdgcn_mov_dpp(f2u(x), CTRL, 0xf, 0xf, true)); }
template <int CTRL> __device__ __forceinline__ unsigned dppu(unsigned x) { return (unsigned)__builtin_amdgcn_mov_dpp((int)x, CTRL, 0xf, 0xf, true); }
constexpr int ROR8 = 0x128, ROR4 = 0x124, ROR2 = 0x122, ROR1 = 0x121;
__device__ __forceinline__ float red32(float a, float b) { auto r = __builtin_amdgcn_permlane32_swap(f2u(a), f2u(b), false, false); return u2f(r[0]) + u2f(r[1]); }
__device__ __forceinline__ float red16(float a, float b) { auto r = __builtin_amdgcn_permlane16_swap(f2u(a), f2u(b), false, false); return u2f(r[0]) + u2f(r[1]); }
__device__ __forceinline__ float mx2(float a, float b) { return fmaxf(a, b); }
__device__ __forceinline__ float mx3(float a, float b, float c) { return fmaxf(fmaxf(a, b), c); }
__device__ __forceinline__ float max32(float a) { auto r = __builtin_amdgcn_permlane32_swap(f2u(a), f2u(a), false, false); return mx2(u2f(r[0]), u2f(r[1])); }
__device__ __forceinline__ float max16(float a) { auto r = __builtin_amdgcn_permlane16_swap(f2u(a), f2u(a), false, false); return mx2(u2f(r[0]), u2f(r[1])); }
__device__ __forceinline__ float dsel(unsigned p, unsigned sel, float acc) { return __builtin_amdgcn_fdot2_f32_bf16(__builtin_bit_cast(b2, p), __builtin_bit_cast(b2, sel), acc, false); }
__device__ __forceinline__ f2 mixp(f2 h, float w1, float b1, float w2, f2 acc) {
    const float c0 = -2.0f * 0.7978845608028654f * 1.4426950408889634f, c1 = c0 * 0.044715f;
    const f2 u = h * w1 + b1;
    const f2 z = u * (u * u * c1 + c0);
    const f2 d = f2{__builtin_amdgcn_exp2f(z.x), __builtin_amdgcn_exp2f(z.y)} + 1.0f;
    const f2 gq = u * f2{__builtin_amdgcn_rcpf(d.x), __builtin_amdgcn_rcpf(d.y)};
    return gq * w2 + acc;
}
__device__ __forceinline__ bool anynan(float x) { return __any(x != x); }
#define PIN(x) asm volatile("" : "+v"(x))
__device__ __forceinline__ float rowsum(float x) { x += dpp<ROR8>(x); x += dpp<ROR4>(x); x += dpp<ROR2>(x); x += dpp<ROR1>(x); return x; }
#define BAR() asm volatile("s_waitcnt lgkmcnt(0)\n\ts_barrier" ::: "memory")
#define CFENCE() asm volatile("" ::: "memory")
#define SB() __builtin_amdgcn_sched_barrier(0)
template <int N> __device__ __forceinline__ void nap() {
#pragma unroll
    for (int i = 0; i < N / 8; ++i) __builtin_amdgcn_s_sleep(8);
    if (N % 8) __builtin_amdgcn_s_sleep(N % 8);
}
__device__ __forceinline__ bool vok(v4u a) { return a.x != INV && a.y != INV && a.z != INV && a.w != INV; }

__device__ __forceinline__ float gelu(float u) {
    const float c0 = -2.0f * 0.7978845608028654f * 1.4426950408889634f, c1 = c0 * 0.044715f;
    float e = __builtin_amdgcn_exp2f(u * fmaf(u * u, c1, c0));
    return u * __builtin_amdgcn_rcpf(1.0f + e);
}
__device__ __forceinline__ float mix2(float h, float w1a, float w1b, float b1a, float b1b, unsigned w2p, float acc) {
    unsigned m = pk(h * w1a, h * w1b);
    unsigned u = pk(bflo(m) + b1a, bfhi(m) + b1b);
    unsigned gq = pk(gelu(bflo(u)), gelu(bfhi(u)));
    return __builtin_amdgcn_fdot2_f32_bf16(__builtin_bit_cast(b2, gq), __builtin_bit_cast(b2, w2p), acc, false);
}

struct Args {
    const u16 *x, *kc, *vc, *wq, *wo, *w1, *b1, *w2, *b2;
    u16* out;
    unsigned char* ws;
};

#define TIMER "s_memrealtime"
#define STAMP(i) do { unsigned long long t_; asm volatile(TIMER " %0\n\ts_waitcnt lgkmcnt(0)" : "=s"(t_) :: "memory"); unsigned tl_ = (unsigned)t_; asm volatile("v_writelane_b32 %0, %1, %2" : "+v"(vst) : "s"(tl_), "i"(i)); } while (0)
#define FST(i) STAMP(i)
#define FLUSH() do { ((int*)(a.ws + WS_STAMP))[(b * 8 + w) * 64 + lane] = vst; } while (0)

__global__ void __launch_bounds__(512, 2) fused(Args a) {
    extern __shared__ __attribute__((aligned(16))) unsigned char smem[];
    LAS unsigned char* L = (LAS unsigned char*)smem;
    const int b = blockIdx.x, tid = threadIdx.x;
    v4u cpf, cpf2;
    {
        unsigned long long pc;
        unsigned co, co2;
        asm volatile("s_getpc_b64 %4\n\tv_lshlrev_b32 %2, 4, %5\n\tv_min_u32 %3, %6, %2\n\tv_add_u32 %3, %7, %3\n\tglobal_load_dwordx4 %0, %2, %4\n\tglobal_load_dwordx4 %1, %3, %4"
                     : "=&v"(cpf), "=&v"(cpf2), "=&v"(co), "=&v"(co2), "=&s"(pc) : "v"(tid), "i"(CODE_PF - 8192 - 16), "i"(8192) : "memory");
        SB();
    }
    const int w = __builtin_amdgcn_readfirstlane(tid >> 6), lane = tid & 63;
    const int w3 = w & 3;
    const int g = b & 7, r = b >> 3;
    const int kb = r & 7, hq = r >> 3, hp = r >> 4, cb = r & 15;
    const int n = lane & 15, q = lane >> 4;
    const __amdgpu_buffer_rsrc_t rs = __builtin_amdgcn_make_buffer_rsrc((void*)a.ws, 0, WS_END, 0x00020000);
    auto ld16 = [&](int off) -> v4u { return __builtin_amdgcn_raw_buffer_load_b128(rs, off, 0, 16); };
    auto ld4 = [&](int off) -> unsigned { return __builtin_amdgcn_raw_buffer_load_b32(rs, off, 0, 16); };
    auto st16 = [&](int off, v4u v) { __builtin_amdgcn_raw_buffer_store_b128(v, rs, off, 0, 16); };
    auto st4 = [&](int off, unsigned v) { __builtin_amdgcn_raw_buffer_store_b32(v, rs, off, 0, 16); };
    const unsigned long long t_start = __builtin_amdgcn_s_memrealtime();
    auto bump = [&](bool tok) {
        const unsigned long long t = __builtin_amdgcn_s_memrealtime();
        if (lane == 0 || (lane == 1 && r == 0 && tok))
            __builtin_amdgcn_raw_buffer_store_b64(__builtin_bit_cast(v2u, t), rs, lane == 0 ? WS_R + g * 512 + r * 16 + 8 : WS_R + 4096 + g * 128, 0, 16);
    };
    auto within = [&](unsigned lo, unsigned hi, unsigned long long lim) -> bool { return (((unsigned long long)hi << 32) | lo) - t_start <= lim - t_start; };
    volatile LAS unsigned* gate = (volatile LAS unsigned*)(L + L_GATE);
    int vst = 0; (void)vst;
    STAMP(0);
    if (w == 0) *(LAS v4u*)(L + L_GATE) = v4u{0u, 0u, 0u, 0u};

    float mu, rsig;
    auto ln_stats = [&](f4 xa, f4 xb) {
        float s = ((xa.x + xa.y) + (xa.z + xa.w)) + ((xb.x + xb.y) + (xb.z + xb.w));
        s = red32(s, s); s = red16(s, s); s = rowsum(s);
        mu = s * (1.0f / 512.0f);
        const f4 da = xa - mu, db = xb - mu;
        float v = ((da.x * da.x + da.y * da.y) + (da.z * da.z + da.w * da.w)) + ((db.x * db.x + db.y * db.y) + (db.z * db.z + db.w * db.w));
        v = red32(v, v); v = red16(v, v); v = rowsum(v);
        rsig = __builtin_amdgcn_rsqf(v * (1.0f / 512.0f) + 1e-5f);
    };

    if (w < 4) {
        const size_t kvb = ((size_t)g * SK + (SK - WIN) + r * 128 + w * 4) * 128;
        const __amdgpu_buffer_rsrc_t rk = __builtin_amdgcn_make_buffer_rsrc((void*)(a.kc + kvb), 0, 32768 - 3072, 0x00020000);
        const __amdgpu_buffer_rsrc_t rv = __builtin_amdgcn_make_buffer_rsrc((void*)(a.vc + kvb), 0, 32768 - 3072, 0x00020000);
        const __amdgpu_buffer_rsrc_t ro = __builtin_amdgcn_make_buffer_rsrc((void*)(a.wo + ((size_t)(g * 2 + hp) * 256 + w * 64) * 512 + cb * 32), 0, 64 * 1024 - 960, 0x00020000);
        unsigned sel_lo = 0x00003f80u, sel_hi = 0x3f800000u;
        asm volatile("" : "+s"(sel_lo), "+s"(sel_hi));
        const unsigned mk0 = (n >> 2) == 0 ? ~0u : 0u, mk1 = (n >> 2) == 1 ? ~0u : 0u, mk2 = (n >> 2) == 2 ? ~0u : 0u, mk3 = (n >> 2) == 3 ? ~0u : 0u;
        LAS unsigned char* stg = L + L_STG + w * 8192;
        const float b2v = bflo(a.b2[0]);
        const v4u x0v = *(const v4u*)(a.x + lane * 8);
        BAR();
        nap<D_K>();
        v4u sb[20];
        auto issue = [&](int j) {
            if (j < 8) sb[j] = __builtin_amdgcn_raw_buffer_load_b128(rk, j * 4096 + lane * 16, 0, 2);
            else if (j < 16) sb[j] = __builtin_amdgcn_raw_buffer_load_b128(rv, (j - 8) * 4096 + lane * 16, 0, 2);
            else if (j < 20) sb[j] = __builtin_amdgcn_raw_buffer_load_b128(ro, ((j - 16) * 16 + (lane >> 2)) * 1024 + (lane & 3) * 16, 0, 2);
            SB();
        };
#pragma unroll
        for (int j = 0; j < WN; ++j) issue(j);
        {
            const f4 xa = {bflo(x0v.x), bfhi(x0v.x), bflo(x0v.y), bfhi(x0v.y)}, xb = {bflo(x0v.z), bfhi(x0v.z), bflo(x0v.w), bfhi(x0v.w)};
            ln_stats(xa, xb);
            if (w == 2) { *(LAS f4*)(L + L_XS + lane * 32) = xa; *(LAS f4*)(L + L_XS + lane * 32 + 16) = xb; }
        }
        asm volatile("" :: "v"(cpf), "v"(cpf2));
        STAMP(1);
#pragma unroll
        for (int i = 0; i < 8; ++i) {
            const int row = i * 4 + q; *(LAS v4u*)(stg + row * 256 + ((n ^ (row & 15)) * 16)) = sb[i];
            SB();
            issue(i + WN);
        }
        STAMP(3);
        v4u kf[2][4], vf[8], wof[2][2];
#pragma unroll
        for (int t = 0; t < 2; ++t)
#pragma unroll
            for (int ks = 0; ks < 4; ++ks) kf[t][ks] = *(LAS v4u*)(stg + (t * 16 + n) * 256 + (((ks * 4 + q) ^ n) * 16));
        SB();
#pragma unroll
        for (int i = 0; i < VH; ++i) {
            const int row = i * 4 + q; *(LAS v4u*)(stg + row * 256 + ((n ^ ((row & 7) << 1)) * 16)) = sb[8 + i];
            SB();
            issue(8 + i + WN);
        }

#pragma unroll
        for (int layer = 0; layer < 2; ++layer) {
            STAMP(4 + layer * 12);
            BAR();
            if (layer == 1) FST(32);
            {
                v4u qf[4];
#pragma unroll
                for (int ks = 0; ks < 4; ++ks) qf[ks] = *(LAS v4u*)(L + L_QS + (n & 3) * 256 + ks * 64 + q * 16);
                SB();
                f4 s0 = {0, 0, 0, 0}, s1 = {0, 0, 0, 0};
#pragma unroll
                for (int ks = 0; ks < 4; ++ks) { s0 = mfma32(kf[0][ks], qf[ks], s0); s1 = mfma32(kf[1][ks], qf[ks], s1); }
                const float C2 = 0.08838834764831845f * 1.4426950408889634f;
                s0 *= C2; s1 *= C2;
                float m = fmaxf(fmaxf(fmaxf(s0.x, s0.y), fmaxf(s0.z, s0.w)), fmaxf(fmaxf(s1.x, s1.y), fmaxf(s1.z, s1.w)));
                m = max32(max16(m));
                float e[8];
                e[0] = __builtin_amdgcn_exp2f(s0.x - m); e[1] = __builtin_amdgcn_exp2f(s0.y - m);
                e[2] = __builtin_amdgcn_exp2f(s0.z - m); e[3] = __builtin_amdgcn_exp2f(s0.w - m);
                e[4] = __builtin_amdgcn_exp2f(s1.x - m); e[5] = __builtin_amdgcn_exp2f(s1.y - m);
                e[6] = __builtin_amdgcn_exp2f(s1.z - m); e[7] = __builtin_amdgcn_exp2f(s1.w - m);
                const v4u pf = {pk(e[0], e[1]), pk(e[2], e[3]), pk(e[4], e[5]), pk(e[6], e[7])};
                STAMP(5 + layer * 12);
                if (layer == 0) {
#pragma unroll
                    for (int i = VH; i < 8; ++i) {
                        const int row = i * 4 + q; *(LAS v4u*)(stg + row * 256 + ((n ^ ((row & 7) << 1)) * 16)) = sb[8 + i];
                        SB();
                        issue(8 + i + WN);
                    }
                    const int row = q * 4 + (n >> 2), p = n & 3;
#pragma unroll
                    for (int c = 0; c < 8; ++c) {
                        LAS unsigned char* ap = stg + row * 256 + (((2 * c + (p >> 1)) ^ ((row & 7) << 1)) * 16) + 8 * (p & 1);
                        const v2u t0 = trd(ap), t1 = trd(ap + 4096);
                        vf[c] = v4u{t0.x, t0.y, t1.x, t1.y};
                    }
                }
                STAMP(6 + layer * 12);
                f4 oa = {0, 0, 0, 0}, ob = {0, 0, 0, 0};
                { const v4u pm = pf & mk0; oa = mfma32(vf[0], pm, oa); ob = mfma32(vf[4], pm, ob); }
                { const v4u pm = pf & mk1; oa = mfma32(vf[1], pm, oa); ob = mfma32(vf[5], pm, ob); }
                { const v4u pm = pf & mk2; oa = mfma32(vf[2], pm, oa); ob = mfma32(vf[6], pm, ob); }
                { const v4u pm = pf & mk3; oa = mfma32(vf[3], pm, oa); ob = mfma32(vf[7], pm, ob); }
                float ls = ((e[0] + e[1]) + (e[2] + e[3])) + ((e[4] + e[5]) + (e[6] + e[7]));
                ls = red32(ls, ls); ls = red16(ls, ls);
                if (lane < 4) { *(LAS float*)(L + L_M + n * 32 + w * 4) = m; *(LAS float*)(L + L_M + n * 32 + 16 + w * 4) = ls; }
                *(LAS f4*)(L + L_OW + (w * 4 + (n & 3)) * 528 + (n >> 2) * 64 + q * 16) = oa;
                *(LAS f4*)(L + L_OW + (w * 4 + (n & 3)) * 528 + (n >> 2) * 64 + 256 + q * 16) = ob;
            }
            if (layer == 1) FST(35);
            BAR();
            if (layer == 1) FST(36);
            {
                int o0 = WS_EC + layer * 262144 + g * 32768 + hp * 16384 + w * 4096 + n * 64 + q * 16;
                int l0 = WS_EL + layer * 65536 + g * 8192 + (hp * 2 + (w >> 1)) * 2048 + q * 256;
                int wa = L_WR + w * 128 + (q >> 1) * 64 + n * 4;
                PIN(o0); PIN(l0); PIN(wa);
                if (layer == 0) {
#pragma unroll
                    for (int i = 0; i < 4; ++i) { const int row = i * 16 + (lane >> 2);
                        *(LAS v4u*)(stg + row * 64 + (((lane & 3) ^ (((row >> 3) & 1) << 1)) * 16)) = sb[16 + i]; }
                    const int aa = n >> 2, p = n & 3;
#pragma unroll
                    for (int ks = 0; ks < 2; ++ks)
#pragma unroll
                        for (int ct = 0; ct < 2; ++ct) {
                            v2u t[2];
#pragma unroll
                            for (int tt = 0; tt < 2; ++tt) { const int row = ks * 32 + q * 8 + tt * 4 + aa;
                                t[tt] = trd(stg + row * 64 + (((2 * ct + (p >> 1)) ^ (((row >> 3) & 1) << 1)) * 16) + 8 * (p & 1)); }
                            wof[ks][ct] = v4u{t[0].x, t[0].y, t[1].x, t[1].y};
                        }
                    nap<D_EC0>();
                } else nap<D_EC>();
                float s;
                for (int it = 0;; ++it) {
                    if (layer == 0 || it > 0) {
                        for (int i2 = 0;; ++i2) {
                            CFENCE();
                            const unsigned f0 = ld4(l0 - q * 256 + (lane & 31) * 64);
                            if (__all(f0 != INV)) break;
                            if (layer == 0 && (i2 & 7) == 7) bump(true);
                            if (i2 > SPIN_CAP) break;
                            nap<4>();
                        }
                    }
                    CFENCE();
                    v4u of[2][2];
                    unsigned le[8];
#pragma unroll
                    for (int j = 0; j < 8; ++j) le[j] = ld4(l0 + (j >> 2) * 1024 + (j & 3) * 64);
#pragma unroll
                    for (int ks = 0; ks < 2; ++ks)
#pragma unroll
                        for (int st = 0; st < 2; ++st) of[ks][st] = ld16(o0 + ks * 2048 + st * 1024);
                    STAMP(8 + layer * 12);
                    float M = mx3(mx3(u2f(le[0]), u2f(le[1]), u2f(le[2])), mx3(u2f(le[3]), u2f(le[4]), u2f(le[5])), mx2(u2f(le[6]), u2f(le[7])));
                    M = max32(max16(M));
                    float e[8];
#pragma unroll
                    for (int j = 0; j < 8; ++j) e[j] = __builtin_amdgcn_exp2f(u2f(le[j]) - M);
                    float Ls = ((e[0] + e[1]) + (e[2] + e[3])) + ((e[4] + e[5]) + (e[6] + e[7]));
                    Ls = red32(Ls, Ls); Ls = red16(Ls, Ls);
                    f4 acc[2][2];
#pragma unroll
                    for (int st = 0; st < 2; ++st)
#pragma unroll
                        for (int ct = 0; ct < 2; ++ct) acc[st][ct] = mfma32(of[1][st], wof[1][ct], mfma32(of[0][st], wof[0][ct], f4{0, 0, 0, 0}));
                    float pc[2];
#pragma unroll
                    for (int ct = 0; ct < 2; ++ct)
                        pc[ct] = ((e[0] * acc[0][ct].x + e[1] * acc[0][ct].y) + (e[2] * acc[0][ct].z + e[3] * acc[0][ct].w)) +
                                 ((e[4] * acc[1][ct].x + e[5] * acc[1][ct].y) + (e[6] * acc[1][ct].z + e[7] * acc[1][ct].w));
                    s = red32(pc[0], pc[1]);
                    s = red16(s, s) * __builtin_amdgcn_rcpf(Ls);
                    if (!anynan(s)) break;
                    bool ok = vok(of[0][0]) && vok(of[0][1]) && vok(of[1][0]) && vok(of[1][1]);
#pragma unroll
                    for (int j = 0; j < 8; ++j) ok = ok && le[j] != INV;
                    if (__all(ok)) break;
                    if (layer == 0 && (it & 7) == 7) bump(true);
                    if (it > SPIN_CAP) break;
                }
                CFENCE();
                if (layer == 1) FST(39);
                if ((q & 1) == 0) *(LAS float*)(L + wa) = s;
            }
            BAR();
            if (layer == 1) FST(40);
            STAMP(9 + layer * 12);
            if (layer == 1 && b != 0) { FLUSH(); return; }
            {
                const int d0 = w * 128 + n * 8 + (q >> 1) * 4 + (q & 1) * 2;
                float x1a, x1b;
                const f2 xp = *(LAS f2*)(L + L_XS + d0 * 4);
                int o0 = WS_EA + layer * 16384 + q * 1024 + w * 256 + n * 16;
                PIN(o0);
                nap<D_EA>();
                for (int it = 0;; ++it) {
                    CFENCE();
                    v4u e[4];
#pragma unroll
                    for (int i = 0; i < 4; ++i) e[i] = ld16(o0 + i * 4096);
                    STAMP(10 + layer * 12);
                    float v[8];
#pragma unroll
                    for (int j = 0; j < 4; ++j) {
                        v[2 * j] = dsel(e[3][j], sel_lo, dsel(e[2][j], sel_lo, dsel(e[1][j], sel_lo, bflo(e[0][j]))));
                        v[2 * j + 1] = dsel(e[3][j], sel_hi, dsel(e[2][j], sel_hi, dsel(e[1][j], sel_hi, bfhi(e[0][j]))));
                    }
                    const float z0 = red16(red32(v[0], v[4]), red32(v[2], v[6])), z1 = red16(red32(v[1], v[5]), red32(v[3], v[7]));
                    const unsigned at = pk(z0, z1);
                    const unsigned x1p = pk(xp.x + bflo(at), xp.y + bfhi(at));
                    x1a = bflo(x1p); x1b = bfhi(x1p);
                    if (!anynan(x1a + x1b)) break;
                    if (__all(vok(e[0]) && vok(e[1]) && vok(e[2]) && vok(e[3]))) break;
                    if (layer == 0 && (it & 7) == 7) bump(true);
                    if (it > SPIN_CAP) break;
                }
                CFENCE();
                {
                    const float da = x1a - mu, db = x1b - mu;
                    float s = red32(da + db, da * da + db * db);
                    s = rowsum(red16(s, s));
                    if ((lane & 31) == 0) *(LAS float*)(L + L_ST1 + w * 8 + (lane >> 5) * 4) = s;
                }
                if (layer == 0) FST(42);
                BAR();
                if (layer == 0) FST(43);
                const f4 va = *(LAS f4*)(L + L_ST1), vb = *(LAS f4*)(L + L_ST1 + 16);
                const float S1 = ((va.x + va.z) + (vb.x + vb.z)) * (1.0f / 512.0f), S2 = (va.y + va.w) + (vb.y + vb.w);
                const float mu1 = mu + S1;
                const float var = mx2(fmaf(S2, 1.0f / 512.0f, -S1 * S1), 0.f);
                const float r1 = __builtin_amdgcn_rsqf(var + 1e-5f);
                const float rr = r1 * __builtin_amdgcn_rsqf(fmaf(var * r1, r1, 1e-5f));
                const unsigned hb = pk((x1a - mu1) * rr, (x1b - mu1) * rr);
                const unsigned ta = (hb & 0x7fffu) - (TE0 << 7), tb = ((hb >> 16) & 0x7fffu) - (TE0 << 7);
                const bool ina = ta < 2048u, inb = tb < 2048u;
                const unsigned ia = (ta & 2047u) | ((hb >> 4) & 2048u), ib = (tb & 2047u) | ((hb >> 20) & 2048u);
                float ya = bflo(*(LAS u16*)(L + L_TAB + ia * 2)), yb = bflo(*(LAS u16*)(L + L_TAB + ib * 2));
                if (__any(!(ina && inb))) {
#pragma nounroll
                    for (int half = 0; half < 2; ++half) {
                        unsigned long long miss = __ballot(half ? !inb : !ina);
                        while (miss) {
                            const int src = __builtin_ctzll(miss);
                            miss &= miss - 1;
                            const unsigned hs = (unsigned)__builtin_amdgcn_readlane((int)hb, src);
                            const float h = half ? bfhi(hs) : bflo(hs);
                            f2 ac2 = {0.f, 0.f};
#pragma nounroll
                            for (int f = lane; f < NF; f += 64)
                                ac2 = mixp(f2{h, h}, bflo(*(LAS u16*)(L + L_SO + f * 2)), bflo(*(LAS u16*)(L + L_SO + 4096 + f * 2)), bflo(*(LAS u16*)(L + L_SO + 8192 + f * 2)), ac2);
                            float acc = ac2.x;
                            acc = red32(acc, acc); acc = rowsum(red16(acc, acc));
                            const float yy = bfr(bfr(acc) + b2v);
                            if (lane == src) { if (half) yb = yy; else ya = yy; }
                        }
                    }
                }
                const unsigned x2p = pk(x1a + ya, x1b + yb);
                if (layer == 1) {
                    *(unsigned*)(a.out + d0) = x2p;
                    STAMP(11 + layer * 12); FLUSH();
                    return;
                }
                *(LAS unsigned*)(L + L_XB + d0 * 2) = x2p;
                *(LAS f2*)(L + L_XS + d0 * 4) = f2{bflo(x2p), bfhi(x2p)};
                FST(45);
                BAR();
                FST(46);
                STAMP(11 + layer * 12);
                mu = mu1;
            }
        }
    } else {
        const __amdgpu_buffer_rsrc_t rq = __builtin_amdgcn_make_buffer_rsrc((void*)(a.wq + (size_t)kb * 64 * 4096 + (g * 4 + hq) * 128), 0, 64 * 8192 - 7936, 0x00020000);
        const int ro_ = lane < 32 ? WS_R + g * 512 + lane * 16 : WS_R + 4096 + (lane & 7) * 128;
        v4u kst[8];
#pragma unroll
        for (int i = 0; i < 4; ++i) kst[i] = __builtin_amdgcn_raw_buffer_load_b128(rq, (w3 * 16 + i * 4 + q) * 8192 + n * 16, 0, 2);
        v4u xf[2];
#pragma unroll
        for (int ks = 0; ks < 2; ++ks) xf[ks] = *(const v4u*)(a.x + kb * 64 + ks * 32 + q * 8);
        const v4u x0v = *(const v4u*)(a.x + lane * 8);
        if (w < 7) {
            const __amdgpu_buffer_rsrc_t rp = __builtin_amdgcn_make_buffer_rsrc((void*)(w == 4 ? a.w1 : w == 5 ? a.b1 : a.w2), 0, NF * 2, 0x00020000);
#pragma unroll
            for (int i = 0; i < 4; ++i) kst[4 + i] = __builtin_amdgcn_raw_buffer_load_b128(rp, i * 1024 + lane * 16, 0, 0);
        } else {
            const v4u iv = {INV, INV, INV, INV};
            st16(WS_EB + (lane >> 5) * 131072 + g * 16384 + kb * 2048 + hq * 512 + (lane & 31) * 16, iv);
            int o;
            if (lane < 32) o = WS_EB + 2 * 131072 + g * 16384 + kb * 2048 + hq * 512 + lane * 16;
            else if (lane < 40) o = WS_EA + ((lane >> 2) & 1) * 16384 + (g * 2 + hp) * 1024 + cb * 64 + (lane & 3) * 16;
            else if (lane < 48) o = WS_EL + ((lane >> 2) & 1) * 65536 + g * 8192 + (lane & 3) * 2048 + r * 64;
            else o = WS_TAB + b * 32 + (lane & 1) * 16;
            if (lane < 50) st16(o, iv);
            st16(WS_EC + g * 32768 + (lane >> 2) * 2048 + r * 64 + (lane & 3) * 16, iv);
            st16(WS_EC + 262144 + g * 32768 + (lane >> 2) * 2048 + r * 64 + (lane & 3) * 16, iv);
        }
        const float b2v = bflo(a.b2[0]);
        BAR();
#pragma unroll
        for (int i = 0; i < 4; ++i) { const int row = w3 * 16 + i * 4 + q;
            *(LAS v4u*)(L + L_SQ + row * 256 + ((n ^ (((row & 3) << 2) | ((row >> 2) & 3))) * 16)) = kst[i]; }
        if (w < 7) {
#pragma unroll
            for (int i = 0; i < 4; ++i) *(LAS v4u*)(L + L_SO + w3 * 4096 + i * 1024 + lane * 16) = kst[4 + i];
        } else {
            asm volatile("s_waitcnt vmcnt(0)" ::: "memory");
            const v2u t = __builtin_bit_cast(v2u, (unsigned long long)__builtin_amdgcn_s_memrealtime());
            if (lane == 0) st16(WS_R + g * 512 + r * 16, v4u{t.x, t.y, t.x, t.y});
        }
        if (n == 1) { xf[0] = v4u{0x3f803f80u, 0x3f803f80u, 0x3f803f80u, 0x3f803f80u}; xf[1] = xf[0]; }
        asm volatile("" :: "v"(cpf), "v"(cpf2));
        STAMP(1);
        asm volatile("s_waitcnt lgkmcnt(0)" ::: "memory");
        if (lane == 0) __hip_atomic_fetch_add((LAS unsigned*)(L + L_GATE), 1u, __ATOMIC_RELAXED, __HIP_MEMORY_SCOPE_WORKGROUP);
        while (*gate < 4u) __builtin_amdgcn_s_sleep(1);
        CFENCE();
        v4u wqf[2][2];
        {
            const int aa = n >> 2, p = n & 3;
#pragma unroll
            for (int ct = 0; ct < 2; ++ct)
#pragma unroll
                for (int ks = 0; ks < 2; ++ks) {
                    v2u t[2];
#pragma unroll
                    for (int tt = 0; tt < 2; ++tt) { const int row = ks * 32 + q * 8 + tt * 4 + aa, ch = 4 * w3 + 2 * ct + (p >> 1);
                        t[tt] = trd(L + L_SQ + row * 256 + ((ch ^ (((row & 3) << 2) | ((row >> 2) & 3))) * 16) + 8 * (p & 1)); }
                    wqf[ct][ks] = v4u{t[0].x, t[0].y, t[1].x, t[1].y};
                }
#pragma unroll
            for (int ct = 0; ct < 2; ++ct) {
                const f4 c = mfma32(wqf[ct][1], xf[1], mfma32(wqf[ct][0], xf[0], f4{0, 0, 0, 0}));
                if (n < 2) st16(WS_EB + n * 2 * 131072 + g * 16384 + kb * 2048 + hq * 512 + w3 * 128 + ct * 64 + q * 16, __builtin_bit_cast(v4u, c));
            }
        }
        SB();
        STAMP(2);
        f2 csum = {0.f, 0.f};
        auto fetch_q = [&](int layer, auto&& meanwhile) {
            int o0 = WS_EB + layer * 131072 + g * 16384 + (lane >> 5) * 2048 + w3 * 512 + (lane & 31) * 16;
            int qa = L_QS + (w3 * 128 + (lane & 31) * 4 + (lane >> 5) * 2) * 2;
            PIN(o0); PIN(qa);
            f2 qv;
            if (layer == 1) nap<D_EB>();
            bool ready = layer == 1;
            for (int it = 0;; ++it) {
                CFENCE();
                v4u e[4], c[4], rec = {0, 0, 0, 0};
                unsigned long long lim = 0;
                if (layer == 0) { lim = __builtin_amdgcn_s_memrealtime() - MARGIN; SB(); if (!ready && lane < 32) rec = ld16(ro_); }
#pragma unroll
                for (int i = 0; i < 4; ++i) e[i] = ld16(o0 + i * 4096);
                if (layer == 0) {
#pragma unroll
                    for (int i = 0; i < 4; ++i) c[i] = ld16(o0 + 2 * 131072 + i * 4096);
                    SB();
                    if (it == 0) meanwhile();
                    SB();
                    const f4 cs = (__builtin_bit_cast(f4, c[0]) + __builtin_bit_cast(f4, c[1])) + (__builtin_bit_cast(f4, c[2]) + __builtin_bit_cast(f4, c[3]));
                    csum = f2{red32(cs.x, cs.z), red32(cs.y, cs.w)};
                    if (!ready) ready = __all(lane >= 32 || within(rec.x, rec.y, lim) || within(rec.z, rec.w, lim));
                }
                const f4 es = (__builtin_bit_cast(f4, e[0]) + __builtin_bit_cast(f4, e[1])) + (__builtin_bit_cast(f4, e[2]) + __builtin_bit_cast(f4, e[3]));
                qv = (f2{red32(es.x, es.z), red32(es.y, es.w)} - csum * mu) * rsig;
                if (ready) {
                    if (!anynan(qv.x + qv.y)) break;
                    bool ok = vok(e[0]) && vok(e[1]) && vok(e[2]) && vok(e[3]);
                    if (layer == 0) ok = ok && vok(c[0]) && vok(c[1]) && vok(c[2]) && vok(c[3]);
                    if (__all(ok)) break;
                }
                if (layer == 0 && (it & 7) == 7) bump(false);
                if (it > SPIN_CAP) break;
            }
            CFENCE();
            *(LAS unsigned*)(L + qa) = pk(qv.x, qv.y);
        };
        float w1f[7], b1f[7], w2f[7];
            f2 tacc[8];
            const unsigned hbits0 = (unsigned)((b >> 7) << 15) | (unsigned)((TE0 << 7) + (b & 127) * 16);
            auto tab_pair = [&](int e) {
                const f2 h = {u2f((hbits0 + e) << 16), u2f((hbits0 + e + 8) << 16)};
                f2 acc = {0.f, 0.f};
#pragma unroll
                for (int j = 0; j < 7; ++j) acc = mixp(h, w1f[j], b1f[j], w2f[j], acc);
                asm volatile("" : "+v"(acc));
                tacc[e] = acc;
            };
#pragma unroll
            for (int j = 0; j < 7; ++j) { const int f = ((tid & 255) + 256 * j) * 2;
                w1f[j] = bflo(*(LAS u16*)(L + L_SO + f)); b1f[j] = bflo(*(LAS u16*)(L + L_SO + 4096 + f)); w2f[j] = bflo(*(LAS u16*)(L + L_SO + 8192 + f)); }
            ln_stats(f4{bflo(x0v.x), bfhi(x0v.x), bflo(x0v.y), bfhi(x0v.y)}, f4{bflo(x0v.z), bfhi(x0v.z), bflo(x0v.w), bfhi(x0v.w)});
            SB();
#pragma unroll
            for (int e = 0; e < TG; ++e) tab_pair(e);
            SB();
            STAMP(3);
            fetch_q(0, [&]() {
#pragma unroll
                for (int e = TG; e < TA; ++e) tab_pair(e);
            });
            if (w == 7) bump(true);
            STAMP(4);
            BAR();
#pragma unroll
            for (int e = TA; e < TB2; ++e) tab_pair(e);
            STAMP(5);
#pragma unroll
        for (int layer = 0; layer < 2; ++layer) {
            if (layer == 1) BAR();
            int ea = WS_EC + layer * 262144 + g * 32768 + (w3 >> 1) * 16384 + ((w3 & 1) * 4 + q) * 2048 + r * 64 + (n >> 2) * 16;
            int oa = L_OW + w3 * 528 + lane * 8;
            PIN(ea); PIN(oa);
            BAR();
            if (layer == 1) FST(36);
            {
                const f4 ma = *(LAS f4*)(L + L_M + w3 * 32), la = *(LAS f4*)(L + L_M + w3 * 32 + 16);
                f2 v[4];
#pragma unroll
                for (int ww = 0; ww < 4; ++ww) v[ww] = *(LAS f2*)(L + oa + ww * 2112);
                const float M = mx2(mx3(ma.x, ma.y, ma.z), ma.w);
                const float e0 = __builtin_amdgcn_exp2f(ma.x - M), e1 = __builtin_amdgcn_exp2f(ma.y - M), e2 = __builtin_amdgcn_exp2f(ma.z - M), e3 = __builtin_amdgcn_exp2f(ma.w - M);
                const float Ls = (e0 * la.x + e1 * la.y) + (e2 * la.z + e3 * la.w);
                const f2 oo = (v[0] * e0 + v[1] * e1) + (v[2] * e2 + v[3] * e3);
                const float inv = __builtin_amdgcn_rcpf(Ls);
                const unsigned wd = nn(pk(oo.x * inv, oo.y * inv));
                const v4u vv = {wd, dppu<0x55>(wd), dppu<0xAA>(wd), dppu<0xFF>(wd)};
                if ((lane & 3) == 0) st16(ea, vv);
                if (lane == 0) st4(WS_EL + layer * 65536 + g * 8192 + w3 * 2048 + r * 64, f2u(M + __builtin_amdgcn_logf(Ls)));
            }
            STAMP(7 + layer * 12);
            if (layer == 0) {
#pragma unroll
                for (int e = TB2; e < 8; ++e) tab_pair(e);
                float t8[8], t4[4];
#pragma unroll
                for (int j = 0; j < 8; ++j) t8[j] = red32(tacc[j].x, tacc[j].y);
#pragma unroll
                for (int j = 0; j < 4; ++j) t4[j] = rowsum(red16(t8[j], t8[j + 4]));
                if (n == 0) *(LAS f4*)(L + L_TR + w3 * 64 + q * 16) = f4{t4[0], t4[1], t4[2], t4[3]};
                STAMP(6);
                if (w == 7) {
                    for (int it = 0;; ++it) {
                        CFENCE();
                        v4u cur = {0, 0, 0, 0};
                        if (lane >= 32) cur = ld16(ro_);
                        if (__all(lane < 32 || within(cur.x, cur.y, __builtin_amdgcn_s_memrealtime()))) break;
                        if ((it & 3) == 3) bump(true);
                        if (it > SPIN_CAP) break;
                        nap<16>();
                    }
                    CFENCE();
                }
            }
            BAR();
            if (w == 4 && lane < 16) {
                f2 v[4];
#pragma unroll
                for (int ww = 0; ww < 4; ++ww) v[ww] = *(LAS f2*)(L + L_WR + ww * 128 + lane * 8);
                const f2 sv = (v[0] + v[1]) + (v[2] + v[3]);
                const unsigned wd = nn(pk(sv.x, sv.y));
                const v4u vv = {wd, dppu<0x55>(wd), dppu<0xAA>(wd), dppu<0xFF>(wd)};
                if ((lane & 3) == 0) st16(WS_EA + layer * 16384 + (g * 2 + hp) * 1024 + cb * 64 + lane * 4, vv);
            }
            if (layer == 0 && w == 5 && lane < 8) {
                f2 tv[4];
#pragma unroll
                for (int ww = 0; ww < 4; ++ww) tv[ww] = *(LAS f2*)(L + L_TR + ww * 64 + lane * 8);
                const f2 sv = (tv[0] + tv[1]) + (tv[2] + tv[3]);
                const unsigned wd = nn(pk(bfr(sv.x) + b2v, bfr(sv.y) + b2v));
                const v4u vv = {wd, dppu<0x55>(wd), dppu<0xAA>(wd), dppu<0xFF>(wd)};
                if ((lane & 3) == 0) st16(WS_TAB + b * 32 + lane * 4, vv);
            }
            STAMP(9 + layer * 12);
            if (layer == 1) { FLUSH(); return; }
            nap<D_EA>();
            for (int it = 0;; ++it) {
                CFENCE();
                const v4u t0 = ld16(WS_TAB + w3 * 2048 + lane * 16), t1 = ld16(WS_TAB + w3 * 2048 + 1024 + lane * 16);
                *(LAS v4u*)(L + L_TAB + w3 * 2048 + lane * 16) = t0; *(LAS v4u*)(L + L_TAB + w3 * 2048 + 1024 + lane * 16) = t1;
                if (__all(vok(t0) && vok(t1))) break;
                if ((it & 7) == 7) bump(true);
                if (it > SPIN_CAP) break;
            }
            CFENCE();
            int qo = WS_EB + 131072 + g * 16384 + kb * 2048 + hq * 512 + w3 * 128 + n * 64 + q * 16;
            int xa_ = L_XB + kb * 128 + q * 16;
            PIN(qo); PIN(xa_);
            BAR();
            BAR();
            {
                const v4u xa = *(LAS v4u*)(L + xa_), xb = *(LAS v4u*)(L + xa_ + 64);
                const f4 c0 = mfma32(wqf[0][1], xb, mfma32(wqf[0][0], xa, f4{0, 0, 0, 0})), c1 = mfma32(wqf[1][1], xb, mfma32(wqf[1][0], xa, f4{0, 0, 0, 0}));
                if (n < 2) st16(qo, __builtin_bit_cast(v4u, n == 0 ? c0 : c1));
            }
            STAMP(11 + layer * 12);
            SB();
            ln_stats(*(LAS f4*)(L + L_XS + lane * 32), *(LAS f4*)(L + L_XS + lane * 32 + 16));
            fetch_q(1, []() {});
            STAMP(16);
        }
    }
}

__global__ void dump(const int* st) {
    if (threadIdx.x != 0) return;
    int base = st[0];
    for (int w = 0; w < 2048; ++w) if (st[w * 64] - base < 0) base = st[w * 64];
    for (int set = 0; set < 2; ++set) {
        printf("set%d:", set);
        for (int i = 0; i < 24; ++i) { int mn = 1 << 30, mx = -1; for (int w = 0; w < 2048; ++w) if (((w & 7) >> 2) == set && st[w * 64 + i]) { int v = st[w * 64 + i] - base; mn = min(mn, v); mx = max(mx, v); }
            if (mx >= 0) printf(" %d:%d-%d", i, mn, mx); }
        printf("\n");
    }
}
}

extern "C" void kernel_launch(void* const* d_in, const int* in_sizes, int n_in,
                              void* d_out, int out_size, void* d_ws, size_t ws_size,
                              hipStream_t stream) {
    fz::Args a;
    a.x = (const u16*)d_in[0]; a.kc = (const u16*)d_in[1]; a.vc = (const u16*)d_in[2];
    a.wq = (const u16*)d_in[3]; a.wo = (const u16*)d_in[4];
    a.w1 = (const u16*)d_in[6]; a.b1 = (const u16*)d_in[7]; a.w2 = (const u16*)d_in[8]; a.b2 = (const u16*)d_in[9];
    a.out = (u16*)d_out; a.ws = (unsigned char*)d_ws;
    (void)hipFuncSetAttribute((const void*)fz::fused, hipFuncAttributeMaxDynamicSharedMemorySize, fz::LDS_BYTES);
    fz::fused<<<256, 512, fz::LDS_BYTES, stream>>>(a);
    fz::dump<<<1, 64, 0, stream>>>((const int*)(a.ws + fz::WS_STAMP));
}
```

```cpp
#define STAMPS 3
#include <hip/hip_runtime.h>
#include <hip/hip_bf16.h>
#include <stdint.h>

typedef unsigned short u16;
typedef unsigned v4u __attribute__((ext_vector_type(4)));
typedef unsigned v2u __attribute__((ext_vector_type(2)));
typedef float f4 __attribute__((ext_vector_type(4)));
typedef float f2 __attribute__((ext_vector_type(2)));
typedef short s4 __attribute__((ext_vector_type(4)));
typedef __bf16 b8 __attribute__((ext_vector_type(8)));
typedef __bf16 b2 __attribute__((ext_vector_type(2)));
#define LAS __attribute__((address_space(3)))
#define GAS __attribute__((address_space(1)))

#define D_EB 8
#define D_EC 24
#define D_EA 20
#define CODE_PF 16384
#define TA 3
#define TB 12
#define D_K 8
#define D_EC0 4
#define TG0 2
#define TG 1
#define VH 4
#define WN 4
#define TB2 5

namespace fz {

constexpr int SK = 65536, WIN = 4096, NF = 1792;
constexpr unsigned INV = 0xFFFFFFFFu;
constexpr int WS_R = 0;
constexpr int WS_TAB = 8192;
constexpr int WS_EB = 16384;
constexpr int WS_EC = WS_EB + 3 * 131072;
constexpr int WS_EL = WS_EC + 2 * 262144;
constexpr int WS_EA = WS_EL + 2 * 65536;
constexpr int WS_END = WS_EA + 2 * 16384;
constexpr int WS_STAMP = 2 << 20;
constexpr int L_STG = 0;
constexpr int L_SQ = 32768;
constexpr int L_SO = L_SQ + 16384;
constexpr int L_TAB = L_SO + 16384;
constexpr int L_OW = L_TAB + 8192;
constexpr int L_QS = L_OW + 4 * 4 * 132 * 4;
constexpr int L_XB = L_QS + 1024;
constexpr int L_XS = L_XB + 1024;
constexpr int L_M = L_XS + 2048;
constexpr int L_WR = L_M + 128;
constexpr int L_ST1 = L_WR + 512;
constexpr int L_ST2 = L_ST1 + 64;
constexpr int L_TR = L_ST2 + 64;
constexpr int L_GATE = L_TR + 512;
constexpr int L_END = L_GATE + 16;
constexpr int LDS_BYTES = 100 * 1024;
static_assert(L_END <= LDS_BYTES, "lds");
constexpr int TE0 = 115;
constexpr int SPIN_CAP = 1 << 22;
constexpr unsigned long long MARGIN = 20;

__device__ __forceinline__ float u2f(unsigned u) { return __builtin_bit_cast(float, u); }
__device__ __forceinline__ unsigned f2u(float f) { return __builtin_bit_cast(unsigned, f); }
__device__ __forceinline__ float bflo(unsigned u) { return u2f(u << 16); }
__device__ __forceinline__ float bfhi(unsigned u) { return u2f(u & 0xffff0000u); }
__device__ __forceinline__ float bfr(float x) { return (float)(__bf16)x; }
__device__ __forceinline__ unsigned pk(float lo, float hi) { b2 v = {(__bf16)lo, (__bf16)hi}; return __builtin_bit_cast(unsigned, v); }
__device__ __forceinline__ unsigned nn(unsigned w) { return w == INV ? 0x7fc07fc0u : w; }
__device__ __forceinline__ f4 mfma32(v4u a, v4u b, f4 c) { return __builtin_amdgcn_mfma_f32_16x16x32_bf16(__builtin_bit_cast(b8, a), __builtin_bit_cast(b8, b), c, 0, 0, 0); }
__device__ __forceinline__ f4 mfma16(v2u a, v2u b, f4 c) { return __builtin_amdgcn_mfma_f32_16x16x16bf16_1k(__builtin_bit_cast(s4, a), __builtin_bit_cast(s4, b), c, 0, 0, 0); }
__device__ __forceinline__ v2u trd(LAS unsigned char* p) { return __builtin_bit_cast(v2u, __builtin_amdgcn_ds_read_tr16_b64_v4i16((LAS s4*)p)); }
template <int CTRL> __device__ __forceinline__ float dpp(float x) { return u2f(__builtin_amdgcn_mov_dpp(f2u(x), CTRL, 0xf, 0xf, true)); }
template <int CTRL> __device__ __forceinline__ unsigned dppu(unsigned x) { return (unsigned)__builtin_amdgcn_mov_dpp((int)x, CTRL, 0xf, 0xf, true); }
constexpr int ROR8 = 0x128, ROR4 = 0x124, ROR2 = 0x122, ROR1 = 0x121;
__device__ __forceinline__ float red32(float a, float b) { auto r = __builtin_amdgcn_permlane32_swap(f2u(a), f2u(b), false, false); return u2f(r[0]) + u2f(r[1]); }
__device__ __forceinline__ float red16(float a, float b) { auto r = __builtin_amdgcn_permlane16_swap(f2u(a), f2u(b), false, false); return u2f(r[0]) + u2f(r[1]); }
__device__ __forceinline__ float mx2(float a, float b) { return fmaxf(a, b); }
__device__ __forceinline__ float mx3(float a, float b, float c) { return fmaxf(fmaxf(a, b), c); }
__device__ __forceinline__ float max32(float a) { auto r = __builtin_amdgcn_permlane32_swap(f2u(a), f2u(a), false, false); return mx2(u2f(r[0]), u2f(r[1])); }
__device__ __forceinline__ float max16(float a) { auto r = __builtin_amdgcn_permlane16_swap(f2u(a), f2u(a), false, false); return mx2(u2f(r[0]), u2f(r[1])); }
__device__ __forceinline__ float dsel(unsigned p, unsigned sel, float acc) { return __builtin_amdgcn_fdot2_f32_bf16(__builtin_bit_cast(b2, p), __builtin_bit_cast(b2, sel), acc, false); }
__device__ __forceinline__ f2 mixp(f2 h, float w1, float b1, float w2, f2 acc) {
    const float c0 = -2.0f * 0.7978845608028654f * 1.4426950408889634f, c1 = c0 * 0.044715f;
    const f2 u = h * w1 + b1;
    const f2 z = u * (u * u * c1 + c0);
    const f2 d = f2{__builtin_amdgcn_exp2f(z.x), __builtin_amdgcn_exp2f(z.y)} + 1.0f;
    const f2 gq = u * f2{__builtin_amdgcn_rcpf(d.x), __builtin_amdgcn_rcpf(d.y)};
    return gq * w2 + acc;
}
__device__ __forceinline__ bool anynan(float x) { return __any(x != x); }
#define PIN(x) asm volatile("" : "+v"(x))
__device__ __forceinline__ float rowsum(float x) { x += dpp<ROR8>(x); x += dpp<ROR4>(x); x += dpp<ROR2>(x); x += dpp<ROR1>(x); return x; }
#define BAR() asm volatile("s_waitcnt lgkmcnt(0)\n\ts_barrier" ::: "memory")
#define CFENCE() asm volatile("" ::: "memory")
#define SB() __builtin_amdgcn_sched_barrier(0)
template <int N> __device__ __forceinline__ void nap() {
#pragma unroll
    for (int i = 0; i < N / 8; ++i) __builtin_amdgcn_s_sleep(8);
    if (N % 8) __builtin_amdgcn_s_sleep(N % 8);
}
__device__ __forceinline__ bool vok(v4u a) { return a.x != INV && a.y != INV && a.z != INV && a.w != INV; }

__device__ __forceinline__ float gelu(float u) {
    const float c0 = -2.0f * 0.7978845608028654f * 1.4426950408889634f, c1 = c0 * 0.044715f;
    float e = __builtin_amdgcn_exp2f(u * fmaf(u * u, c1, c0));
    return u * __builtin_amdgcn_rcpf(1.0f + e);
}
__device__ __forceinline__ float mix2(float h, float w1a, float w1b, float b1a, float b1b, unsigned w2p, float acc) {
    unsigned m = pk(h * w1a, h * w1b);
    unsigned u = pk(bflo(m) + b1a, bfhi(m) + b1b);
    unsigned gq = pk(gelu(bflo(u)), gelu(bfhi(u)));
    return __builtin_amdgcn_fdot2_f32_bf16(__builtin_bit_cast(b2, gq), __builtin_bit_cast(b2, w2p), acc, false);
}

struct Args {
    const u16 *x, *kc, *vc, *wq, *wo, *w1, *b1, *w2, *b2;
    u16* out;
    unsigned char* ws;
};

#define TIMER "s_memrealtime"
#define STAMP(i) do { unsigned long long t_; asm volatile(TIMER " %0\n\ts_waitcnt lgkmcnt(0)" : "=s"(t_) :: "memory"); unsigned tl_ = (unsigned)t_; asm volatile("v_writelane_b32 %0, %1, %2" : "+v"(vst) : "s"(tl_), "i"(i)); } while (0)
#define FST(i) STAMP(i)
#define FLUSH() do { ((int*)(a.ws + WS_STAMP))[(b * 8 + w) * 64 + lane] = vst; } while (0)

__global__ void __launch_bounds__(512, 2) fused(Args a) {
    extern __shared__ __attribute__((aligned(16))) unsigned char smem[];
    LAS unsigned char* L = (LAS unsigned char*)smem;
    const int b = blockIdx.x, tid = threadIdx.x;
    v4u cpf, cpf2;
    {
        unsigned long long pc;
        unsigned co, co2;
        asm volatile("s_getpc_b64 %4\n\tv_lshlrev_b32 %2, 4, %5\n\tv_min_u32 %3, %6, %2\n\tv_add_u32 %3, %7, %3\n\tglobal_load_dwordx4 %0, %2, %4\n\tglobal_load_dwordx4 %1, %3, %4"
                     : "=&v"(cpf), "=&v"(cpf2), "=&v"(co), "=&v"(co2), "=&s"(pc) : "v"(tid), "i"(CODE_PF - 8192 - 16), "i"(8192) : "memory");
        SB();
    }
    const int w = __builtin_amdgcn_readfirstlane(tid >> 6), lane = tid & 63;
    const int w3 = w & 3;
    const int g = b & 7, r = b >> 3;
    const int kb = r & 7, hq = r >> 3, hp = r >> 4, cb = r & 15;
    const int n = lane & 15, q = lane >> 4;
    const __amdgpu_buffer_rsrc_t rs = __builtin_amdgcn_make_buffer_rsrc((void*)a.ws, 0, WS_END, 0x00020000);
    auto ld16 = [&](int off) -> v4u { return __builtin_amdgcn_raw_buffer_load_b128(rs, off, 0, 16); };
    auto ld4 = [&](int off) -> unsigned { return __builtin_amdgcn_raw_buffer_load_b32(rs, off, 0, 16); };
    auto st16 = [&](int off, v4u v) { __builtin_amdgcn_raw_buffer_store_b128(v, rs, off, 0, 16); };
    auto st4 = [&](int off, unsigned v) { __builtin_amdgcn_raw_buffer_store_b32(v, rs, off, 0, 16); };
    const unsigned long long t_start = __builtin_amdgcn_s_memrealtime();
    auto bump = [&](bool tok) {
        const unsigned long long t = __builtin_amdgcn_s_memrealtime();
        if (lane == 0 || (lane == 1 && r == 0 && tok))
            __builtin_amdgcn_raw_buffer_store_b64(__builtin_bit_cast(v2u, t), rs, lane == 0 ? WS_R + g * 512 + r * 16 + 8 : WS_R + 4096 + g * 128, 0, 16);
    };
    auto within = [&](unsigned lo, unsigned hi, unsigned long long lim) -> bool { return (((unsigned long long)hi << 32) | lo) - t_start <= lim - t_start; };
    volatile LAS unsigned* gate = (volatile LAS unsigned*)(L + L_GATE);
    int vst = 0; (void)vst;
    STAMP(0);
    if (w == 0) *(LAS v4u*)(L + L_GATE) = v4u{0u, 0u, 0u, 0u};

    float mu, rsig;
    auto ln_stats = [&](f4 xa, f4 xb) {
        float s = ((xa.x + xa.y) + (xa.z + xa.w)) + ((xb.x + xb.y) + (xb.z + xb.w));
        s = red32(s, s); s = red16(s, s); s = rowsum(s);
        mu = s * (1.0f / 512.0f);
        const f4 da = xa - mu, db = xb - mu;
        float v = ((da.x * da.x + da.y * da.y) + (da.z * da.z + da.w * da.w)) + ((db.x * db.x + db.y * db.y) + (db.z * db.z + db.w * db.w));
        v = red32(v, v); v = red16(v, v); v = rowsum(v);
        rsig = __builtin_amdgcn_rsqf(v * (1.0f / 512.0f) + 1e-5f);
    };

    if (w < 4) {
        const size_t kvb = ((size_t)g * SK + (SK - WIN) + r * 128 + w * 4) * 128;
        const __amdgpu_buffer_rsrc_t rk = __builtin_amdgcn_make_buffer_rsrc((void*)(a.kc + kvb), 0, 32768 - 3072, 0x00020000);
        const __amdgpu_buffer_rsrc_t rv = __builtin_amdgcn_make_buffer_rsrc((void*)(a.vc + kvb), 0, 32768 - 3072, 0x00020000);
        const __amdgpu_buffer_rsrc_t ro = __builtin_amdgcn_make_buffer_rsrc((void*)(a.wo + ((size_t)(g * 2 + hp) * 256 + w * 64) * 512 + cb * 32), 0, 64 * 1024 - 960, 0x00020000);
        unsigned sel_lo = 0x00003f80u, sel_hi = 0x3f800000u;
        asm volatile("" : "+s"(sel_lo), "+s"(sel_hi));
        const unsigned mk0 = (n >> 2) == 0 ? ~0u : 0u, mk1 = (n >> 2) == 1 ? ~0u : 0u, mk2 = (n >> 2) == 2 ? ~0u : 0u, mk3 = (n >> 2) == 3 ? ~0u : 0u;
        LAS unsigned char* stg = L + L_STG + w * 8192;
        const float b2v = bflo(a.b2[0]);
        const v4u x0v = *(const v4u*)(a.x + lane * 8);
        BAR();
        nap<D_K>();
        v4u sb[20];
        auto issue = [&](int j) {
            if (j < 8) sb[j] = __builtin_amdgcn_raw_buffer_load_b128(rk, j * 4096 + lane * 16, 0, 2);
            else if (j < 16) sb[j] = __builtin_amdgcn_raw_buffer_load_b128(rv, (j - 8) * 4096 + lane * 16, 0, 2);
            else if (j < 20) sb[j] = __builtin_amdgcn_raw_buffer_load_b128(ro, ((j - 16) * 16 + (lane >> 2)) * 1024 + (lane & 3) * 16, 0, 2);
            SB();
        };
#pragma unroll
        for (int j = 0; j < WN; ++j) issue(j);
        {
            const f4 xa = {bflo(x0v.x), bfhi(x0v.x), bflo(x0v.y), bfhi(x0v.y)}, xb = {bflo(x0v.z), bfhi(x0v.z), bflo(x0v.w), bfhi(x0v.w)};
            ln_stats(xa, xb);
            if (w == 2) { *(LAS f4*)(L + L_XS + lane * 32) = xa; *(LAS f4*)(L + L_XS + lane * 32 + 16) = xb; }
        }
        asm volatile("" :: "v"(cpf), "v"(cpf2));
        STAMP(1);
#pragma unroll
        for (int i = 0; i < 8; ++i) {
            const int row = i * 4 + q; *(LAS v4u*)(stg + row * 256 + ((n ^ (row & 15)) * 16)) = sb[i];
            SB();
            issue(i + WN);
        }
        STAMP(3);
        v4u kf[2][4], vf[8], wof[2][2];
#pragma unroll
        for (int t = 0; t < 2; ++t)
#pragma unroll
            for (int ks = 0; ks < 4; ++ks) kf[t][ks] = *(LAS v4u*)(stg + (t * 16 + n) * 256 + (((ks * 4 + q) ^ n) * 16));
        SB();
#pragma unroll
        for (int i = 0; i < VH; ++i) {
            const int row = i * 4 + q; *(LAS v4u*)(stg + row * 256 + ((n ^ ((row & 7) << 1)) * 16)) = sb[8 + i];
            SB();
            issue(8 + i + WN);
        }

#pragma unroll
        for (int layer = 0; layer < 2; ++layer) {
            STAMP(4 + layer * 12);
            BAR();
            if (layer == 1) FST(32);
            {
                v4u qf[4];
#pragma unroll
                for (int ks = 0; ks < 4; ++ks) qf[ks] = *(LAS v4u*)(L + L_QS + (n & 3) * 256 + ks * 64 + q * 16);
                SB();
                f4 s0 = {0, 0, 0, 0}, s1 = {0, 0, 0, 0};
#pragma unroll
                for (int ks = 0; ks < 4; ++ks) { s0 = mfma32(kf[0][ks], qf[ks], s0); s1 = mfma32(kf[1][ks], qf[ks], s1); }
                const float C2 = 0.08838834764831845f * 1.4426950408889634f;
                s0 *= C2; s1 *= C2;
                float m = fmaxf(fmaxf(fmaxf(s0.x, s0.y), fmaxf(s0.z, s0.w)), fmaxf(fmaxf(s1.x, s1.y), fmaxf(s1.z, s1.w)));
                m = max32(max16(m));
                float e[8];
                e[0] = __builtin_amdgcn_exp2f(s0.x - m); e[1] = __builtin_amdgcn_exp2f(s0.y - m);
                e[2] = __builtin_amdgcn_exp2f(s0.z - m); e[3] = __builtin_amdgcn_exp2f(s0.w - m);
                e[4] = __builtin_amdgcn_exp2f(s1.x - m); e[5] = __builtin_amdgcn_exp2f(s1.y - m);
                e[6] = __builtin_amdgcn_exp2f(s1.z - m); e[7] = __builtin_amdgcn_exp2f(s1.w - m);
                const v4u pf = {pk(e[0], e[1]), pk(e[2], e[3]), pk(e[4], e[5]), pk(e[6], e[7])};
                STAMP(5 + layer * 12);
                if (layer == 0) {
#pragma unroll
                    for (int i = VH; i < 8; ++i) {
                        const int row = i * 4 + q; *(LAS v4u*)(stg + row * 256 + ((n ^ ((row & 7) << 1)) * 16)) = sb[8 + i];
                        SB();
                        issue(8 + i + WN);
                    }
                    const int row = q * 4 + (n >> 2), p = n & 3;
#pragma unroll
                    for (int c = 0; c < 8; ++c) {
                        LAS unsigned char* ap = stg + row * 256 + (((2 * c + (p >> 1)) ^ ((row & 7) << 1)) * 16) + 8 * (p & 1);
                        const v2u t0 = trd(ap), t1 = trd(ap + 4096);
                        vf[c] = v4u{t0.x, t0.y, t1.x, t1.y};
                    }
                }
                STAMP(6 + layer * 12);
                f4 oa = {0, 0, 0, 0}, ob = {0, 0, 0, 0};
                { const v4u pm = pf & mk0; oa = mfma32(vf[0], pm, oa); ob = mfma32(vf[4], pm, ob); }
                { const v4u pm = pf & mk1; oa = mfma32(vf[1], pm, oa); ob = mfma32(vf[5], pm, ob); }
                { const v4u pm = pf & mk2; oa = mfma32(vf[2], pm, oa); ob = mfma32(vf[6], pm, ob); }
                { const v4u pm = pf & mk3; oa = mfma32(vf[3], pm, oa); ob = mfma32(vf[7], pm, ob); }
                float ls = ((e[0] + e[1]) + (e[2] + e[3])) + ((e[4] + e[5]) + (e[6] + e[7]));
                ls = red32(ls, ls); ls = red16(ls, ls);
                if (lane < 4) { *(LAS float*)(L + L_M + n * 32 + w * 4) = m; *(LAS float*)(L + L_M + n * 32 + 16 + w * 4) = ls; }
                *(LAS f4*)(L + L_OW + (w * 4 + (n & 3)) * 528 + (n >> 2) * 64 + q * 16) = oa;
                *(LAS f4*)(L + L_OW + (w * 4 + (n & 3)) * 528 + (n >> 2) * 64 + 256 + q * 16) = ob;
            }
            if (layer == 1) FST(35);
            BAR();
            if (layer == 1) FST(36);
            {
                int o0 = WS_EC + layer * 262144 + g * 32768 + hp * 16384 + w * 4096 + n * 64 + q * 16;
                int l0 = WS_EL + layer * 65536 + g * 8192 + (hp * 2 + (w >> 1)) * 2048 + q * 256;
                int wa = L_WR + w * 128 + (q >> 1) * 64 + n * 4;
                PIN(o0); PIN(l0); PIN(wa);
                if (layer == 0) {
#pragma unroll
                    for (int i = 0; i < 4; ++i) { const int row = i * 16 + (lane >> 2);
                        *(LAS v4u*)(stg + row * 64 + (((lane & 3) ^ (((row >> 3) & 1) << 1)) * 16)) = sb[16 + i]; }
                    const int aa = n >> 2, p = n & 3;
#pragma unroll
                    for (int ks = 0; ks < 2; ++ks)
#pragma unroll
                        for (int ct = 0; ct < 2; ++ct) {
                            v2u t[2];
#pragma unroll
                            for (int tt = 0; tt < 2; ++tt) { const int row = ks * 32 + q * 8 + tt * 4 + aa;
                                t[tt] = trd(stg + row * 64 + (((2 * ct + (p >> 1)) ^ (((row >> 3) & 1) << 1)) * 16) + 8 * (p & 1)); }
                            wof[ks][ct] = v4u{t[0].x, t[0].y, t[1].x, t[1].y};
                        }
                    nap<D_EC0>();
                } else nap<D_EC>();
                float s;
                for (int it = 0;; ++it) {
                    if (layer == 0 || it > 0) {
                        for (int i2 = 0;; ++i2) {
                            CFENCE();
                            const unsigned f0 = ld4(l0 - q * 256 + (lane & 31) * 64);
                            if (__all(f0 != INV)) break;
                            if (layer == 0 && (i2 & 7) == 7) bump(true);
                            if (i2 > SPIN_CAP) break;
                            nap<4>();
                        }
                    }
                    CFENCE();
                    v4u of[2][2];
                    unsigned le[8];
#pragma unroll
                    for (int j = 0; j < 8; ++j) le[j] = ld4(l0 + (j >> 2) * 1024 + (j & 3) * 64);
#pragma unroll
                    for (int ks = 0; ks < 2; ++ks)
#pragma unroll
                        for (int st = 0; st < 2; ++st) of[ks][st] = ld16(o0 + ks * 2048 + st * 1024);
                    STAMP(8 + layer * 12);
                    float M = mx3(mx3(u2f(le[0]), u2f(le[1]), u2f(le[2])), mx3(u2f(le[3]), u2f(le[4]), u2f(le[5])), mx2(u2f(le[6]), u2f(le[7])));
                    M = max32(max16(M));
                    float e[8];
#pragma unroll
                    for (int j = 0; j < 8; ++j) e[j] = __builtin_amdgcn_exp2f(u2f(le[j]) - M);
                    float Ls = ((e[0] + e[1]) + (e[2] + e[3])) + ((e[4] + e[5]) + (e[6] + e[7]));
                    Ls = red32(Ls, Ls); Ls = red16(Ls, Ls);
                    f4 acc[2][2];
#pragma unroll
                    for (int st = 0; st < 2; ++st)
#pragma unroll
                        for (int ct = 0; ct < 2; ++ct) acc[st][ct] = mfma32(of[1][st], wof[1][ct], mfma32(of[0][st], wof[0][ct], f4{0, 0, 0, 0}));
                    float pc[2];
#pragma unroll
                    for (int ct = 0; ct < 2; ++ct)
                        pc[ct] = ((e[0] * acc[0][ct].x + e[1] * acc[0][ct].y) + (e[2] * acc[0][ct].z + e[3] * acc[0][ct].w)) +
                                 ((e[4] * acc[1][ct].x + e[5] * acc[1][ct].y) + (e[6] * acc[1][ct].z + e[7] * acc[1][ct].w));
                    s = red32(pc[0], pc[1]);
                    s = red16(s, s) * __builtin_amdgcn_rcpf(Ls);
                    if (!anynan(s)) break;
                    bool ok = vok(of[0][0]) && vok(of[0][1]) && vok(of[1][0]) && vok(of[1][1]);
#pragma unroll
                    for (int j = 0; j < 8; ++j) ok = ok && le[j] != INV;
                    if (__all(ok)) break;
                    if (layer == 0 && (it & 7) == 7) bump(true);
                    if (it > SPIN_CAP) break;
                }
                CFENCE();
                if (layer == 1) FST(39);
                if ((q & 1) == 0) *(LAS float*)(L + wa) = s;
            }
            BAR();
            if (layer == 1) FST(40);
            STAMP(9 + layer * 12);
            if (layer == 1 && b != 0) { FLUSH(); return; }
            {
                const int d0 = w * 128 + n * 8 + (q >> 1) * 4 + (q & 1) * 2;
                float x1a, x1b;
                const f2 xp = *(LAS f2*)(L + L_XS + d0 * 4);
                int o0 = WS_EA + layer * 16384 + q * 1024 + w * 256 + n * 16;
                PIN(o0);
                nap<D_EA>();
                for (int it = 0;; ++it) {
                    CFENCE();
                    v4u e[4];
#pragma unroll
                    for (int i = 0; i < 4; ++i) e[i] = ld16(o0 + i * 4096);
                    STAMP(10 + layer * 12);
                    float v[8];
#pragma unroll
                    for (int j = 0; j < 4; ++j) {
                        v[2 * j] = dsel(e[3][j], sel_lo, dsel(e[2][j], sel_lo, dsel(e[1][j], sel_lo, bflo(e[0][j]))));
                        v[2 * j + 1] = dsel(e[3][j], sel_hi, dsel(e[2][j], sel_hi, dsel(e[1][j], sel_hi, bfhi(e[0][j]))));
                    }
                    const float z0 = red16(red32(v[0], v[4]), red32(v[2], v[6])), z1 = red16(red32(v[1], v[5]), red32(v[3], v[7]));
                    const unsigned at = pk(z0, z1);
                    const unsigned x1p = pk(xp.x + bflo(at), xp.y + bfhi(at));
                    x1a = bflo(x1p); x1b = bfhi(x1p);
                    if (!anynan(x1a + x1b)) break;
                    if (__all(vok(e[0]) && vok(e[1]) && vok(e[2]) && vok(e[3]))) break;
                    if (layer == 0 && (it & 7) == 7) bump(true);
                    if (it > SPIN_CAP) break;
                }
                CFENCE();
                {
                    const float da = x1a - mu, db = x1b - mu;
                    float s = red32(da + db, da * da + db * db);
                    s = rowsum(red16(s, s));
                    if ((lane & 31) == 0) *(LAS float*)(L + L_ST1 + w * 8 + (lane >> 5) * 4) = s;
                }
                if (layer == 0) FST(42);
                BAR();
                if (layer == 0) FST(43);
                const f4 va = *(LAS f4*)(L + L_ST1), vb = *(LAS f4*)(L + L_ST1 + 16);
                const float S1 = ((va.x + va.z) + (vb.x + vb.z)) * (1.0f / 512.0f), S2 = (va.y + va.w) + (vb.y + vb.w);
                const float mu1 = mu + S1;
                const float var = mx2(fmaf(S2, 1.0f / 512.0f, -S1 * S1), 0.f);
                const float r1 = __builtin_amdgcn_rsqf(var + 1e-5f);
                const float rr = r1 * __builtin_amdgcn_rsqf(fmaf(var * r1, r1, 1e-5f));
                const unsigned hb = pk((x1a - mu1) * rr, (x1b - mu1) * rr);
                const unsigned ta = (hb & 0x7fffu) - (TE0 << 7), tb = ((hb >> 16) & 0x7fffu) - (TE0 << 7);
                const bool ina = ta < 2048u, inb = tb < 2048u;
                const unsigned ia = (ta & 2047u) | ((hb >> 4) & 2048u), ib = (tb & 2047u) | ((hb >> 20) & 2048u);
                float ya = bflo(*(LAS u16*)(L + L_TAB + ia * 2)), yb = bflo(*(LAS u16*)(L + L_TAB + ib * 2));
                if (__any(!(ina && inb))) {
#pragma nounroll
                    for (int half = 0; half < 2; ++half) {
                        unsigned long long miss = __ballot(half ? !inb : !ina);
                        while (miss) {
                            const int src = __builtin_ctzll(miss);
                            miss &= miss - 1;
                            const unsigned hs = (unsigned)__builtin_amdgcn_readlane((int)hb, src);
                            const float h = half ? bfhi(hs) : bflo(hs);
                            f2 ac2 = {0.f, 0.f};
#pragma nounroll
                            for (int f = lane; f < NF; f += 64)
                                ac2 = mixp(f2{h, h}, bflo(*(LAS u16*)(L + L_SO + f * 2)), bflo(*(LAS u16*)(L + L_SO + 4096 + f * 2)), bflo(*(LAS u16*)(L + L_SO + 8192 + f * 2)), ac2);
                            float acc = ac2.x;
                            acc = red32(acc, acc); acc = rowsum(red16(acc, acc));
                            const float yy = bfr(bfr(acc) + b2v);
                            if (lane == src) { if (half) yb = yy; else ya = yy; }
                        }
                    }
                }
                const unsigned x2p = pk(x1a + ya, x1b + yb);
                if (layer == 1) {
                    *(unsigned*)(a.out + d0) = x2p;
                    STAMP(11 + layer * 12); FLUSH();
                    return;
                }
                *(LAS unsigned*)(L + L_XB + d0 * 2) = x2p;
                *(LAS f2*)(L + L_XS + d0 * 4) = f2{bflo(x2p), bfhi(x2p)};
                FST(45);
                BAR();
                FST(46);
                STAMP(11 + layer * 12);
                mu = mu1;
            }
        }
    } else {
        const __amdgpu_buffer_rsrc_t rq = __builtin_amdgcn_make_buffer_rsrc((void*)(a.wq + (size_t)kb * 64 * 4096 + (g * 4 + hq) * 128), 0, 64 * 8192 - 7936, 0x00020000);
        const int ro_ = lane < 32 ? WS_R + g * 512 + lane * 16 : WS_R + 4096 + (lane & 7) * 128;
        v4u kst[8];
#pragma unroll
        for (int i = 0; i < 4; ++i) kst[i] = __builtin_amdgcn_raw_buffer_load_b128(rq, (w3 * 16 + i * 4 + q) * 8192 + n * 16, 0, 2);
        v4u xf[2];
#pragma unroll
        for (int ks = 0; ks < 2; ++ks) xf[ks] = *(const v4u*)(a.x + kb * 64 + ks * 32 + q * 8);
        const v4u x0v = *(const v4u*)(a.x + lane * 8);
        if (w < 7) {
            const __amdgpu_buffer_rsrc_t rp = __builtin_amdgcn_make_buffer_rsrc((void*)(w == 4 ? a.w1 : w == 5 ? a.b1 : a.w2), 0, NF * 2, 0x00020000);
#pragma unroll
            for (int i = 0; i < 4; ++i) kst[4 + i] = __builtin_amdgcn_raw_buffer_load_b128(rp, i * 1024 + lane * 16, 0, 0);
        } else {
            const v4u iv = {INV, INV, INV, INV};
            st16(WS_EB + (lane >> 5) * 131072 + g * 16384 + kb * 2048 + hq * 512 + (lane & 31) * 16, iv);
            int o;
            if (lane < 32) o = WS_EB + 2 * 131072 + g * 16384 + kb * 2048 + hq * 512 + lane * 16;
            else if (lane < 40) o = WS_EA + ((lane >> 2) & 1) * 16384 + (g * 2 + hp) * 1024 + cb * 64 + (lane & 3) * 16;
            else if (lane < 48) o = WS_EL + ((lane >> 2) & 1) * 65536 + g * 8192 + (lane & 3) * 2048 + r * 64;
            else o = WS_TAB + b * 32 + (lane & 1) * 16;
            if (lane < 50) st16(o, iv);
            st16(WS_EC + g * 32768 + (lane >> 2) * 2048 + r * 64 + (lane & 3) * 16, iv);
            st16(WS_EC + 262144 + g * 32768 + (lane >> 2) * 2048 + r * 64 + (lane & 3) * 16, iv);
        }
        const float b2v = bflo(a.b2[0]);
        BAR();
#pragma unroll
        for (int i = 0; i < 4; ++i) { const int row = w3 * 16 + i * 4 + q;
            *(LAS v4u*)(L + L_SQ + row * 256 + ((n ^ (((row & 3) << 2) | ((row >> 2) & 3))) * 16)) = kst[i]; }
        if (w < 7) {
#pragma unroll
            for (int i = 0; i < 4; ++i) *(LAS v4u*)(L + L_SO + w3 * 4096 + i * 1024 + lane * 16) = kst[4 + i];
        } else {
            asm volatile("s_waitcnt vmcnt(0)" ::: "memory");
            const v2u t = __builtin_bit_cast(v2u, (unsigned long long)__builtin_amdgcn_s_memrealtime());
            if (lane == 0) st16(WS_R + g * 512 + r * 16, v4u{t.x, t.y, t.x, t.y});
        }
        if (n == 1) { xf[0] = v4u{0x3f803f80u, 0x3f803f80u, 0x3f803f80u, 0x3f803f80u}; xf[1] = xf[0]; }
        asm volatile("" :: "v"(cpf), "v"(cpf2));
        STAMP(1);
        asm volatile("s_waitcnt lgkmcnt(0)" ::: "memory");
        if (lane == 0) __hip_atomic_fetch_add((LAS unsigned*)(L + L_GATE), 1u, __ATOMIC_RELAXED, __HIP_MEMORY_SCOPE_WORKGROUP);
        while (*gate < 4u) __builtin_amdgcn_s_sleep(1);
        CFENCE();
        v4u wqf[2][2];
        {
            const int aa = n >> 2, p = n & 3;
#pragma unroll
            for (int ct = 0; ct < 2; ++ct)
#pragma unroll
                for (int ks = 0; ks < 2; ++ks) {
                    v2u t[2];
#pragma unroll
                    for (int tt = 0; tt < 2; ++tt) { const int row = ks * 32 + q * 8 + tt * 4 + aa, ch = 4 * w3 + 2 * ct + (p >> 1);
                        t[tt] = trd(L + L_SQ + row * 256 + ((ch ^ (((row & 3) << 2) | ((row >> 2) & 3))) * 16) + 8 * (p & 1)); }
                    wqf[ct][ks] = v4u{t[0].x, t[0].y, t[1].x, t[1].y};
                }
#pragma unroll
            for (int ct = 0; ct < 2; ++ct) {
                const f4 c = mfma32(wqf[ct][1], xf[1], mfma32(wqf[ct][0], xf[0], f4{0, 0, 0, 0}));
                if (n < 2) st16(WS_EB + n * 2 * 131072 + g * 16384 + kb * 2048 + hq * 512 + w3 * 128 + ct * 64 + q * 16, __builtin_bit_cast(v4u, c));
            }
        }
        SB();
        STAMP(2);
        f2 csum = {0.f, 0.f};
        auto fetch_q = [&](int layer, auto&& meanwhile) {
            int o0 = WS_EB + layer * 131072 + g * 16384 + (lane >> 5) * 2048 + w3 * 512 + (lane & 31) * 16;
            int qa = L_QS + (w3 * 128 + (lane & 31) * 4 + (lane >> 5) * 2) * 2;
            PIN(o0); PIN(qa);
            f2 qv;
            if (layer == 1) nap<D_EB>();
            bool ready = layer == 1;
            for (int it = 0;; ++it) {
                CFENCE();
                v4u e[4], c[4], rec = {0, 0, 0, 0};
                unsigned long long lim = 0;
                if (layer == 0) { lim = __builtin_amdgcn_s_memrealtime() - MARGIN; SB(); if (!ready && lane < 32) rec = ld16(ro_); }
#pragma unroll
                for (int i = 0; i < 4; ++i) e[i] = ld16(o0 + i * 4096);
                if (layer == 0) {
#pragma unroll
                    for (int i = 0; i < 4; ++i) c[i] = ld16(o0 + 2 * 131072 + i * 4096);
                    SB();
                    if (it == 0) meanwhile();
                    SB();
                    const f4 cs = (__builtin_bit_cast(f4, c[0]) + __builtin_bit_cast(f4, c[1])) + (__builtin_bit_cast(f4, c[2]) + __builtin_bit_cast(f4, c[3]));
                    csum = f2{red32(cs.x, cs.z), red32(cs.y, cs.w)};
                    if (!ready) ready = __all(lane >= 32 || within(rec.x, rec.y, lim) || within(rec.z, rec.w, lim));
                }
                const f4 es = (__builtin_bit_cast(f4, e[0]) + __builtin_bit_cast(f4, e[1])) + (__builtin_bit_cast(f4, e[2]) + __builtin_bit_cast(f4, e[3]));
                qv = (f2{red32(es.x, es.z), red32(es.y, es.w)} - csum * mu) * rsig;
                if (ready) {
                    if (!anynan(qv.x + qv.y)) break;
                    bool ok = vok(e[0]) && vok(e[1]) && vok(e[2]) && vok(e[3]);
                    if (layer == 0) ok = ok && vok(c[0]) && vok(c[1]) && vok(c[2]) && vok(c[3]);
                    if (__all(ok)) break;
                }
                if (layer == 0 && (it & 7) == 7) bump(false);
                if (it > SPIN_CAP) break;
            }
            CFENCE();
            *(LAS unsigned*)(L + qa) = pk(qv.x, qv.y);
        };
        float w1f[7], b1f[7], w2f[7];
            f2 tacc[8];
            const unsigned hbits0 = (unsigned)((b >> 7) << 15) | (unsigned)((TE0 << 7) + (b & 127) * 16);
            auto tab_pair = [&](int e) {
                const f2 h = {u2f((hbits0 + e) << 16), u2f((hbits0 + e + 8) << 16)};
                f2 acc = {0.f, 0.f};
#pragma unroll
                for (int j = 0; j < 7; ++j) acc = mixp(h, w1f[j], b1f[j], w2f[j], acc);
                asm volatile("" : "+v"(acc));
                tacc[e] = acc;
            };
#pragma unroll
            for (int j = 0; j < 7; ++j) { const int f = ((tid & 255) + 256 * j) * 2;
                w1f[j] = bflo(*(LAS u16*)(L + L_SO + f)); b1f[j] = bflo(*(LAS u16*)(L + L_SO + 4096 + f)); w2f[j] = bflo(*(LAS u16*)(L + L_SO + 8192 + f)); }
            ln_stats(f4{bflo(x0v.x), bfhi(x0v.x), bflo(x0v.y), bfhi(x0v.y)}, f4{bflo(x0v.z), bfhi(x0v.z), bflo(x0v.w), bfhi(x0v.w)});
            SB();
#pragma unroll
            for (int e = 0; e < TG; ++e) tab_pair(e);
            SB();
            STAMP(3);
            fetch_q(0, [&]() {
#pragma unroll
                for (int e = TG; e < TA; ++e) tab_pair(e);
            });
            if (w == 7) bump(true);
            STAMP(4);
            BAR();
#pragma unroll
            for (int e = TA; e < TB2; ++e) tab_pair(e);
            STAMP(5);
#pragma unroll
        for (int layer = 0; layer < 2; ++layer) {
            if (layer == 1) BAR();
            int ea = WS_EC + layer * 262144 + g * 32768 + (w3 >> 1) * 16384 + ((w3 & 1) * 4 + q) * 2048 + r * 64 + (n >> 2) * 16;
            int oa = L_OW + w3 * 528 + lane * 8;
            PIN(ea); PIN(oa);
            BAR();
            if (layer == 1) FST(36);
            {
                const f4 ma = *(LAS f4*)(L + L_M + w3 * 32), la = *(LAS f4*)(L + L_M + w3 * 32 + 16);
                f2 v[4];
#pragma unroll
                for (int ww = 0; ww < 4; ++ww) v[ww] = *(LAS f2*)(L + oa + ww * 2112);
                const float M = mx2(mx3(ma.x, ma.y, ma.z), ma.w);
                const float e0 = __builtin_amdgcn_exp2f(ma.x - M), e1 = __builtin_amdgcn_exp2f(ma.y - M), e2 = __builtin_amdgcn_exp2f(ma.z - M), e3 = __builtin_amdgcn_exp2f(ma.w - M);
                const float Ls = (e0 * la.x + e1 * la.y) + (e2 * la.z + e3 * la.w);
                const f2 oo = (v[0] * e0 + v[1] * e1) + (v[2] * e2 + v[3] * e3);
                const float inv = __builtin_amdgcn_rcpf(Ls);
                const unsigned wd = nn(pk(oo.x * inv, oo.y * inv));
                const v4u vv = {wd, dppu<0x55>(wd), dppu<0xAA>(wd), dppu<0xFF>(wd)};
                if ((lane & 3) == 0) st16(ea, vv);
                if (lane == 0) st4(WS_EL + layer * 65536 + g * 8192 + w3 * 2048 + r * 64, f2u(M + __builtin_amdgcn_logf(Ls)));
            }
            STAMP(7 + layer * 12);
            if (layer == 0) {
#pragma unroll
                for (int e = TB2; e < 8; ++e) tab_pair(e);
                float t8[8], t4[4];
#pragma unroll
                for (int j = 0; j < 8; ++j) t8[j] = red32(tacc[j].x, tacc[j].y);
#pragma unroll
                for (int j = 0; j < 4; ++j) t4[j] = rowsum(red16(t8[j], t8[j + 4]));
                if (n == 0) *(LAS f4*)(L + L_TR + w3 * 64 + q * 16) = f4{t4[0], t4[1], t4[2], t4[3]};
                STAMP(6);
                if (w == 7) {
                    for (int it = 0;; ++it) {
                        CFENCE();
                        v4u cur = {0, 0, 0, 0};
                        if (lane >= 32) cur = ld16(ro_);
                        if (__all(lane < 32 || within(cur.x, cur.y, __builtin_amdgcn_s_memrealtime()))) break;
                        if ((it & 3) == 3) bump(true);
                        if (it > SPIN_CAP) break;
                        nap<16>();
                    }
                    CFENCE();
                }
            }
            BAR();
            if (w == 4 && lane < 16) {
                f2 v[4];
#pragma unroll
                for (int ww = 0; ww < 4; ++ww) v[ww] = *(LAS f2*)(L + L_WR + ww * 128 + lane * 8);
                const f2 sv = (v[0] + v[1]) + (v[2] + v[3]);
                const unsigned wd = nn(pk(sv.x, sv.y));
                const v4u vv = {wd, dppu<0x55>(wd), dppu<0xAA>(wd), dppu<0xFF>(wd)};
                if ((lane & 3) == 0) st16(WS_EA + layer * 16384 + (g * 2 + hp) * 1024 + cb * 64 + lane * 4, vv);
            }
            if (layer == 0 && w == 5 && lane < 8) {
                f2 tv[4];
#pragma unroll
                for (int ww = 0; ww < 4; ++ww) tv[ww] = *(LAS f2*)(L + L_TR + ww * 64 + lane * 8);
                const f2 sv = (tv[0] + tv[1]) + (tv[2] + tv[3]);
                const unsigned wd = nn(pk(bfr(sv.x) + b2v, bfr(sv.y) + b2v));
                const v4u vv = {wd, dppu<0x55>(wd), dppu<0xAA>(wd), dppu<0xFF>(wd)};
                if ((lane & 3) == 0) st16(WS_TAB + b * 32 + lane * 4, vv);
            }
            STAMP(9 + layer * 12);
            if (layer == 1) { FLUSH(); return; }
            nap<D_EA>();
            for (int it = 0;; ++it) {
                CFENCE();
                const v4u t0 = ld16(WS_TAB + w3 * 2048 + lane * 16), t1 = ld16(WS_TAB + w3 * 2048 + 1024 + lane * 16);
                *(LAS v4u*)(L + L_TAB + w3 * 2048 + lane * 16) = t0; *(LAS v4u*)(L + L_TAB + w3 * 2048 + 1024 + lane * 16) = t1;
                if (__all(vok(t0) && vok(t1))) break;
                if ((it & 7) == 7) bump(true);
                if (it > SPIN_CAP) break;
            }
            CFENCE();
            int qo = WS_EB + 131072 + g * 16384 + kb * 2048 + hq * 512 + w3 * 128 + n * 64 + q * 16;
            int xa_ = L_XB + kb * 128 + q * 16;
            PIN(qo); PIN(xa_);
            BAR();
            BAR();
            {
                const v4u xa = *(LAS v4u*)(L + xa_), xb = *(LAS v4u*)(L + xa_ + 64);
                const f4 c0 = mfma32(wqf[0][1], xb, mfma32(wqf[0][0], xa, f4{0, 0, 0, 0})), c1 = mfma32(wqf[1][1], xb, mfma32(wqf[1][0], xa, f4{0, 0, 0, 0}));
                if (n < 2) st16(qo, __builtin_bit_cast(v4u, n == 0 ? c0 : c1));
            }
            STAMP(11 + layer * 12);
            SB();
            ln_stats(*(LAS f4*)(L + L_XS + lane * 32), *(LAS f4*)(L + L_XS + lane * 32 + 16));
            fetch_q(1, []() {});
            STAMP(16);
        }
    }
}

__global__ void probe(const int* st) {
    __shared__ int sb[256], sm[256];
    const int t = threadIdx.x;
    int ref = st[0];
    int mn = 0, mx = -(1 << 30);
    for (int w = t; w < 2048; w += 256) { mn = min(mn, st[w * 64] - ref); if (((w & 7) >> 2) == 1 && st[w * 64 + 2]) mx = max(mx, st[w * 64 + 2] - ref); }
    sb[t] = mn; sm[t] = mx; __syncthreads();
    if (t == 0) { for (int i = 0; i < 256; ++i) { mn = min(mn, sb[i]); mx = max(mx, sm[i]); }
        const unsigned long long until = __builtin_amdgcn_s_memrealtime() + (unsigned long long)(mx - mn) * 100ull;
        while (__builtin_amdgcn_s_memrealtime() < until) __builtin_amdgcn_s_sleep(8); }
}
}

extern "C" void kernel_launch(void* const* d_in, const int* in_sizes, int n_in,
                              void* d_out, int out_size, void* d_ws, size_t ws_size,
                              hipStream_t stream) {
    fz::Args a;
    a.x = (const u16*)d_in[0]; a.kc = (const u16*)d_in[1]; a.vc = (const u16*)d_in[2];
    a.wq = (const u16*)d_in[3]; a.wo = (const u16*)d_in[4];
    a.w1 = (const u16*)d_in[6]; a.b1 = (const u16*)d_in[7]; a.w2 = (const u16*)d_in[8]; a.b2 = (const u16*)d_in[9];
    a.out = (u16*)d_out; a.ws = (unsigned char*)d_ws;
    (void)hipFuncSetAttribute((const void*)fz::fused, hipFuncAttributeMaxDynamicSharedMemorySize, fz::LDS_BYTES);
    fz::fused<<<256, 512, fz::LDS_BYTES, stream>>>(a);
    fz::probe<<<1, 256, 0, stream>>>((const int*)(a.ws + fz::WS_STAMP));
}
```

```cpp
#include <hip/hip_runtime.h>
#include <hip/hip_bf16.h>
#include <stdint.h>

typedef unsigned short u16;
typedef unsigned v4u __attribute__((ext_vector_type(4)));
typedef unsigned v2u __attribute__((ext_vector_type(2)));
typedef float f4 __attribute__((ext_vector_type(4)));
typedef float f2 __attribute__((ext_vector_type(2)));
typedef short s4 __attribute__((ext_vector_type(4)));
typedef __bf16 b8 __attribute__((ext_vector_type(8)));
typedef __bf16 b2 __attribute__((ext_vector_type(2)));
#define LAS __attribute__((address_space(3)))
#define GAS __attribute__((address_space(1)))

#define D_EB 8
#define D_EC 24
#define D_EA 20
#define CODE_PF 16384
#define TA 3
#define TB 12
#define D_K 8
#define D_EC0 4
#define TG0 2
#define TG 1
#define VH 4
#define WN 4
#define TB2 5
#define STAMPS 0

namespace fz {

constexpr int SK = 65536, WIN = 4096, NF = 1792;
constexpr unsigned INV = 0xFFFFFFFFu;
constexpr int WS_R = 0;
constexpr int WS_TAB = 8192;
constexpr int WS_EB = 16384;
constexpr int WS_EC = WS_EB + 3 * 131072;
constexpr int WS_EL = WS_EC + 2 * 262144;
constexpr int WS_EA = WS_EL + 2 * 65536;
constexpr int WS_END = WS_EA + 2 * 16384;
constexpr int WS_STAMP = 2 << 20;
constexpr int L_STG = 0;
constexpr int L_SQ = 32768;
constexpr int L_SO = L_SQ + 16384;
constexpr int L_TAB = L_SO + 16384;
constexpr int L_OW = L_TAB + 8192;
constexpr int L_QS = L_OW + 4 * 4 * 132 * 4;
constexpr int L_XB = L_QS + 1024;
constexpr int L_XS = L_XB + 1024;
constexpr int L_M = L_XS + 2048;
constexpr int L_WR = L_M + 128;
constexpr int L_ST1 = L_WR + 512;
constexpr int L_ST2 = L_ST1 + 64;
constexpr int L_TR = L_ST2 + 64;
constexpr int L_GATE = L_TR + 512;
constexpr int L_END = L_GATE + 16;
constexpr int LDS_BYTES = 100 * 1024;
static_assert(L_END <= LDS_BYTES, "lds");
constexpr int TE0 = 115;
constexpr int SPIN_CAP = 1 << 22;
constexpr unsigned long long MARGIN = 20;

__device__ __forceinline__ float u2f(unsigned u) { return __builtin_bit_cast(float, u); }
__device__ __forceinline__ unsigned f2u(float f) { return __builtin_bit_cast(unsigned, f); }
__device__ __forceinline__ float bflo(unsigned u) { return u2f(u << 16); }
__device__ __forceinline__ float bfhi(unsigned u) { return u2f(u & 0xffff0000u); }
__device__ __forceinline__ float bfr(float x) { return (float)(__bf16)x; }
__device__ __forceinline__ unsigned pk(float lo, float hi) { b2 v = {(__bf16)lo, (__bf16)hi}; return __builtin_bit_cast(unsigned, v); }
__device__ __forceinline__ unsigned nn(unsigned w) { return w == INV ? 0x7fc07fc0u : w; }
__device__ __forceinline__ f4 mfma32(v4u a, v4u b, f4 c) { return __builtin_amdgcn_mfma_f32_16x16x32_bf16(__builtin_bit_cast(b8, a), __builtin_bit_cast(b8, b), c, 0, 0, 0); }
__device__ __forceinline__ f4 mfma16(v2u a, v2u b, f4 c) { return __builtin_amdgcn_mfma_f32_16x16x16bf16_1k(__builtin_bit_cast(s4, a), __builtin_bit_cast(s4, b), c, 0, 0, 0); }
__device__ __forceinline__ v2u trd(LAS unsigned char* p) { return __builtin_bit_cast(v2u, __builtin_amdgcn_ds_read_tr16_b64_v4i16((LAS s4*)p)); }
template <int CTRL> __device__ __forceinline__ float dpp(float x) { return u2f(__builtin_amdgcn_mov_dpp(f2u(x), CTRL, 0xf, 0xf, true)); }
template <int CTRL> __device__ __forceinline__ unsigned dppu(unsigned x) { return (unsigned)__builtin_amdgcn_mov_dpp((int)x, CTRL, 0xf, 0xf, true); }
constexpr int ROR8 = 0x128, ROR4 = 0x124, ROR2 = 0x122, ROR1 = 0x121;
__device__ __forceinline__ float red32(float a, float b) { auto r = __builtin_amdgcn_permlane32_swap(f2u(a), f2u(b), false, false); return u2f(r[0]) + u2f(r[1]); }
__device__ __forceinline__ float red16(float a, float b) { auto r = __builtin_amdgcn_permlane16_swap(f2u(a), f2u(b), false, false); return u2f(r[0]) + u2f(r[1]); }
__device__ __forceinline__ float mx2(float a, float b) { return fmaxf(a, b); }
__device__ __forceinline__ float mx3(float a, float b, float c) { return fmaxf(fmaxf(a, b), c); }
__device__ __forceinline__ float max32(float a) { auto r = __builtin_amdgcn_permlane32_swap(f2u(a), f2u(a), false, false); return mx2(u2f(r[0]), u2f(r[1])); }
__device__ __forceinline__ float max16(float a) { auto r = __builtin_amdgcn_permlane16_swap(f2u(a), f2u(a), false, false); return mx2(u2f(r[0]), u2f(r[1])); }
__device__ __forceinline__ float dsel(unsigned p, unsigned sel, float acc) { return __builtin_amdgcn_fdot2_f32_bf16(__builtin_bit_cast(b2, p), __builtin_bit_cast(b2, sel), acc, false); }
__device__ __forceinline__ f2 mixp(f2 h, float w1, float b1, float w2, f2 acc) {
    const float c0 = -2.0f * 0.7978845608028654f * 1.4426950408889634f, c1 = c0 * 0.044715f;
    const f2 u = h * w1 + b1;
    const f2 z = u * (u * u * c1 + c0);
    const f2 d = f2{__builtin_amdgcn_exp2f(z.x), __builtin_amdgcn_exp2f(z.y)} + 1.0f;
    const f2 gq = u * f2{__builtin_amdgcn_rcpf(d.x), __builtin_amdgcn_rcpf(d.y)};
    return gq * w2 + acc;
}
__device__ __forceinline__ bool anynan(float x) { return __any(x != x); }
#define PIN(x) asm volatile("" : "+v"(x))
__device__ __forceinline__ float rowsum(float x) { x += dpp<ROR8>(x); x += dpp<ROR4>(x); x += dpp<ROR2>(x); x += dpp<ROR1>(x); return x; }
#define BAR() asm volatile("s_waitcnt lgkmcnt(0)\n\ts_barrier" ::: "memory")
#define CFENCE() asm volatile("" ::: "memory")
#define SB() __builtin_amdgcn_sched_barrier(0)
template <int N> __device__ __forceinline__ void nap() {
#pragma unroll
    for (int i = 0; i < N / 8; ++i) __builtin_amdgcn_s_sleep(8);
    if (N % 8) __builtin_amdgcn_s_sleep(N % 8);
}
__device__ __forceinline__ bool vok(v4u a) { return a.x != INV && a.y != INV && a.z != INV && a.w != INV; }

__device__ __forceinline__ float gelu(float u) {
    const float c0 = -2.0f * 0.7978845608028654f * 1.4426950408889634f, c1 = c0 * 0.044715f;
    float e = __builtin_amdgcn_exp2f(u * fmaf(u * u, c1, c0));
    return u * __builtin_amdgcn_rcpf(1.0f + e);
}
__device__ __forceinline__ float mix2(float h, float w1a, float w1b, float b1a, float b1b, unsigned w2p, float acc) {
    unsigned m = pk(h * w1a, h * w1b);
    unsigned u = pk(bflo(m) + b1a, bfhi(m) + b1b);
    unsigned gq = pk(gelu(bflo(u)), gelu(bfhi(u)));
    return __builtin_amdgcn_fdot2_f32_bf16(__builtin_bit_cast(b2, gq), __builtin_bit_cast(b2, w2p), acc, false);
}

struct Args {
    const u16 *x, *kc, *vc, *wq, *wo, *w1, *b1, *w2, *b2;
    u16* out;
    unsigned char* ws;
};

#define FST(i) do {} while (0)
#define FLUSH() do {} while (0)
#define STAMP(i) do {} while (0)

__global__ void __launch_bounds__(512, 2) fused(Args a) {
    extern __shared__ __attribute__((aligned(16))) unsigned char smem[];
    LAS unsigned char* L = (LAS unsigned char*)smem;
    const int b = blockIdx.x, tid = threadIdx.x;
    v4u cpf = {0,0,0,0}, cpf2 = {0,0,0,0};
    const int w = __builtin_amdgcn_readfirstlane(tid >> 6), lane = tid & 63;
    const int w3 = w & 3;
    const int g = b & 7, r = b >> 3;
    const int kb = r & 7, hq = r >> 3, hp = r >> 4, cb = r & 15;
    const int n = lane & 15, q = lane >> 4;
    const __amdgpu_buffer_rsrc_t rs = __builtin_amdgcn_make_buffer_rsrc((void*)a.ws, 0, WS_END, 0x00020000);
    auto ld16 = [&](int off) -> v4u { return __builtin_amdgcn_raw_buffer_load_b128(rs, off, 0, 16); };
    auto ld4 = [&](int off) -> unsigned { return __builtin_amdgcn_raw_buffer_load_b32(rs, off, 0, 16); };
    auto st16 = [&](int off, v4u v) { __builtin_amdgcn_raw_buffer_store_b128(v, rs, off, 0, 16); };
    auto st4 = [&](int off, unsigned v) { __builtin_amdgcn_raw_buffer_store_b32(v, rs, off, 0, 16); };
    const unsigned long long t_start = __builtin_amdgcn_s_memrealtime();
    auto bump = [&](bool tok) {
        const unsigned long long t = __builtin_amdgcn_s_memrealtime();
        if (lane == 0 || (lane == 1 && r == 0 && tok))
            __builtin_amdgcn_raw_buffer_store_b64(__builtin_bit_cast(v2u, t), rs, lane == 0 ? WS_R + g * 512 + r * 16 + 8 : WS_R + 4096 + g * 128, 0, 16);
    };
    auto within = [&](unsigned lo, unsigned hi, unsigned long long lim) -> bool { return (((unsigned long long)hi << 32) | lo) - t_start <= lim - t_start; };
    volatile LAS unsigned* gate = (volatile LAS unsigned*)(L + L_GATE);
    int vst = 0; (void)vst;
    STAMP(0);
    if (w == 0) *(LAS v4u*)(L + L_GATE) = v4u{0u, 0u, 0u, 0u};

    float mu, rsig;
    auto ln_stats = [&](f4 xa, f4 xb) {
        float s = ((xa.x + xa.y) + (xa.z + xa.w)) + ((xb.x + xb.y) + (xb.z + xb.w));
        s = red32(s, s); s = red16(s, s); s = rowsum(s);
        mu = s * (1.0f / 512.0f);
        const f4 da = xa - mu, db = xb - mu;
        float v = ((da.x * da.x + da.y * da.y) + (da.z * da.z + da.w * da.w)) + ((db.x * db.x + db.y * db.y) + (db.z * db.z + db.w * db.w));
        v = red32(v, v); v = red16(v, v); v = rowsum(v);
        rsig = __builtin_amdgcn_rsqf(v * (1.0f / 512.0f) + 1e-5f);
    };

    if (w < 4) {
        const size_t kvb = ((size_t)g * SK + (SK - WIN) + r * 128 + w * 4) * 128;
        const __amdgpu_buffer_rsrc_t rk = __builtin_amdgcn_make_buffer_rsrc((void*)(a.kc + kvb), 0, 32768 - 3072, 0x00020000);
        const __amdgpu_buffer_rsrc_t rv = __builtin_amdgcn_make_buffer_rsrc((void*)(a.vc + kvb), 0, 32768 - 3072, 0x00020000);
        const __amdgpu_buffer_rsrc_t ro = __builtin_amdgcn_make_buffer_rsrc((void*)(a.wo + ((size_t)(g * 2 + hp) * 256 + w * 64) * 512 + cb * 32), 0, 64 * 1024 - 960, 0x00020000);
        unsigned sel_lo = 0x00003f80u, sel_hi = 0x3f800000u;
        asm volatile("" : "+s"(sel_lo), "+s"(sel_hi));
        const unsigned mk0 = (n >> 2) == 0 ? ~0u : 0u, mk1 = (n >> 2) == 1 ? ~0u : 0u, mk2 = (n >> 2) == 2 ? ~0u : 0u, mk3 = (n >> 2) == 3 ? ~0u : 0u;
        LAS unsigned char* stg = L + L_STG + w * 8192;
        const float b2v = bflo(a.b2[0]);
        const v4u x0v = *(const v4u*)(a.x + lane * 8);
        BAR();
        nap<D_K>();
        v4u sb[20];
        auto issue = [&](int j) {
            if (j < 8) sb[j] = __builtin_amdgcn_raw_buffer_load_b128(rk, j * 4096 + lane * 16, 0, 2);
            else if (j < 16) sb[j] = __builtin_amdgcn_raw_buffer_load_b128(rv, (j - 8) * 4096 + lane * 16, 0, 2);
            else if (j < 20) sb[j] = __builtin_amdgcn_raw_buffer_load_b128(ro, ((j - 16) * 16 + (lane >> 2)) * 1024 + (lane & 3) * 16, 0, 2);
            SB();
        };
#pragma unroll
        for (int j = 0; j < WN; ++j) issue(j);
        {
            const f4 xa = {bflo(x0v.x), bfhi(x0v.x), bflo(x0v.y), bfhi(x0v.y)}, xb = {bflo(x0v.z), bfhi(x0v.z), bflo(x0v.w), bfhi(x0v.w)};
            ln_stats(xa, xb);
            if (w == 2) { *(LAS f4*)(L + L_XS + lane * 32) = xa; *(LAS f4*)(L + L_XS + lane * 32 + 16) = xb; }
        }
        asm volatile("" :: "v"(cpf), "v"(cpf2));
        STAMP(1);
#pragma unroll
        for (int i = 0; i < 8; ++i) {
            const int row = i * 4 + q; *(LAS v4u*)(stg + row * 256 + ((n ^ (row & 15)) * 16)) = sb[i];
            SB();
            issue(i + WN);
        }
        STAMP(3);
        v4u kf[2][4], vf[8], wof[2][2];
#pragma unroll
        for (int t = 0; t < 2; ++t)
#pragma unroll
            for (int ks = 0; ks < 4; ++ks) kf[t][ks] = *(LAS v4u*)(stg + (t * 16 + n) * 256 + (((ks * 4 + q) ^ n) * 16));
        SB();
#pragma unroll
        for (int i = 0; i < VH; ++i) {
            const int row = i * 4 + q; *(LAS v4u*)(stg + row * 256 + ((n ^ ((row & 7) << 1)) * 16)) = sb[8 + i];
            SB();
            issue(8 + i + WN);
        }

#pragma unroll
        for (int layer = 0; layer < 2; ++layer) {
            STAMP(4 + layer * 12);
            BAR();
            if (layer == 1) FST(32);
            {
                v4u qf[4];
#pragma unroll
                for (int ks = 0; ks < 4; ++ks) qf[ks] = *(LAS v4u*)(L + L_QS + (n & 3) * 256 + ks * 64 + q * 16);
                SB();
                f4 s0 = {0, 0, 0, 0}, s1 = {0, 0, 0, 0};
#pragma unroll
                for (int ks = 0; ks < 4; ++ks) { s0 = mfma32(kf[0][ks], qf[ks], s0); s1 = mfma32(kf[1][ks], qf[ks], s1); }
                const float C2 = 0.08838834764831845f * 1.4426950408889634f;
                s0 *= C2; s1 *= C2;
                float m = fmaxf(fmaxf(fmaxf(s0.x, s0.y), fmaxf(s0.z, s0.w)), fmaxf(fmaxf(s1.x, s1.y), fmaxf(s1.z, s1.w)));
                m = max32(max16(m));
                float e[8];
                e[0] = __builtin_amdgcn_exp2f(s0.x - m); e[1] = __builtin_amdgcn_exp2f(s0.y - m);
                e[2] = __builtin_amdgcn_exp2f(s0.z - m); e[3] = __builtin_amdgcn_exp2f(s0.w - m);
                e[4] = __builtin_amdgcn_exp2f(s1.x - m); e[5] = __builtin_amdgcn_exp2f(s1.y - m);
                e[6] = __builtin_amdgcn_exp2f(s1.z - m); e[7] = __builtin_amdgcn_exp2f(s1.w - m);
                const v4u pf = {pk(e[0], e[1]), pk(e[2], e[3]), pk(e[4], e[5]), pk(e[6], e[7])};
                STAMP(5 + layer * 12);
                if (layer == 0) {
#pragma unroll
                    for (int i = VH; i < 8; ++i) {
                        const int row = i * 4 + q; *(LAS v4u*)(stg + row * 256 + ((n ^ ((row & 7) << 1)) * 16)) = sb[8 + i];
                        SB();
                        issue(8 + i + WN);
                    }
                    const int row = q * 4 + (n >> 2), p = n & 3;
#pragma unroll
                    for (int c = 0; c < 8; ++c) {
                        LAS unsigned char* ap = stg + row * 256 + (((2 * c + (p >> 1)) ^ ((row & 7) << 1)) * 16) + 8 * (p & 1);
                        const v2u t0 = trd(ap), t1 = trd(ap + 4096);
                        vf[c] = v4u{t0.x, t0.y, t1.x, t1.y};
                    }
                }
                STAMP(6 + layer * 12);
                f4 oa = {0, 0, 0, 0}, ob = {0, 0, 0, 0};
                { const v4u pm = pf & mk0; oa = mfma32(vf[0], pm, oa); ob = mfma32(vf[4], pm, ob); }
                { const v4u pm = pf & mk1; oa = mfma32(vf[1], pm, oa); ob = mfma32(vf[5], pm, ob); }
                { const v4u pm = pf & mk2; oa = mfma32(vf[2], pm, oa); ob = mfma32(vf[6], pm, ob); }
                { const v4u pm = pf & mk3; oa = mfma32(vf[3], pm, oa); ob = mfma32(vf[7], pm, ob); }
                float ls = ((e[0] + e[1]) + (e[2] + e[3])) + ((e[4] + e[5]) + (e[6] + e[7]));
                ls = red32(ls, ls); ls = red16(ls, ls);
                if (lane < 4) { *(LAS float*)(L + L_M + n * 32 + w * 4) = m; *(LAS float*)(L + L_M + n * 32 + 16 + w * 4) = ls; }
                *(LAS f4*)(L + L_OW + (w * 4 + (n & 3)) * 528 + (n >> 2) * 64 + q * 16) = oa;
                *(LAS f4*)(L + L_OW + (w * 4 + (n & 3)) * 528 + (n >> 2) * 64 + 256 + q * 16) = ob;
            }
            if (layer == 1) FST(35);
            BAR();
            if (layer == 1) FST(36);
            {
                int o0 = WS_EC + layer * 262144 + g * 32768 + hp * 16384 + w * 4096 + n * 64 + q * 16;
                int l0 = WS_EL + layer * 65536 + g * 8192 + (hp * 2 + (w >> 1)) * 2048 + q * 256;
                int wa = L_WR + w * 128 + (q >> 1) * 64 + n * 4;
                PIN(o0); PIN(l0); PIN(wa);
                if (layer == 0) {
#pragma unroll
                    for (int i = 0; i < 4; ++i) { const int row = i * 16 + (lane >> 2);
                        *(LAS v4u*)(stg + row * 64 + (((lane & 3) ^ (((row >> 3) & 1) << 1)) * 16)) = sb[16 + i]; }
                    const int aa = n >> 2, p = n & 3;
#pragma unroll
                    for (int ks = 0; ks < 2; ++ks)
#pragma unroll
                        for (int ct = 0; ct < 2; ++ct) {
                            v2u t[2];
#pragma unroll
                            for (int tt = 0; tt < 2; ++tt) { const int row = ks * 32 + q * 8 + tt * 4 + aa;
                                t[tt] = trd(stg + row * 64 + (((2 * ct + (p >> 1)) ^ (((row >> 3) & 1) << 1)) * 16) + 8 * (p & 1)); }
                            wof[ks][ct] = v4u{t[0].x, t[0].y, t[1].x, t[1].y};
                        }
                    nap<D_EC0>();
                } else nap<D_EC>();
                float s;
                for (int it = 0;; ++it) {
                    if (layer == 0 || it > 0) {
                        for (int i2 = 0;; ++i2) {
                            CFENCE();
                            const unsigned f0 = ld4(l0 - q * 256 + (lane & 31) * 64);
                            if (__all(f0 != INV)) break;
                            if (layer == 0 && (i2 & 7) == 7) bump(true);
                            if (i2 > SPIN_CAP) break;
                            nap<4>();
                        }
                    }
                    CFENCE();
                    v4u of[2][2];
                    unsigned le[8];
#pragma unroll
                    for (int j = 0; j < 8; ++j) le[j] = ld4(l0 + (j >> 2) * 1024 + (j & 3) * 64);
#pragma unroll
                    for (int ks = 0; ks < 2; ++ks)
#pragma unroll
                        for (int st = 0; st < 2; ++st) of[ks][st] = ld16(o0 + ks * 2048 + st * 1024);
                    STAMP(8 + layer * 12);
                    float M = mx3(mx3(u2f(le[0]), u2f(le[1]), u2f(le[2])), mx3(u2f(le[3]), u2f(le[4]), u2f(le[5])), mx2(u2f(le[6]), u2f(le[7])));
                    M = max32(max16(M));
                    float e[8];
#pragma unroll
                    for (int j = 0; j < 8; ++j) e[j] = __builtin_amdgcn_exp2f(u2f(le[j]) - M);
                    float Ls = ((e[0] + e[1]) + (e[2] + e[3])) + ((e[4] + e[5]) + (e[6] + e[7]));
                    Ls = red32(Ls, Ls); Ls = red16(Ls, Ls);
                    f4 acc[2][2];
#pragma unroll
                    for (int st = 0; st < 2; ++st)
#pragma unroll
                        for (int ct = 0; ct < 2; ++ct) acc[st][ct] = mfma32(of[1][st], wof[1][ct], mfma32(of[0][st], wof[0][ct], f4{0, 0, 0, 0}));
                    float pc[2];
#pragma unroll
                    for (int ct = 0; ct < 2; ++ct)
                        pc[ct] = ((e[0] * acc[0][ct].x + e[1] * acc[0][ct].y) + (e[2] * acc[0][ct].z + e[3] * acc[0][ct].w)) +
                                 ((e[4] * acc[1][ct].x + e[5] * acc[1][ct].y) + (e[6] * acc[1][ct].z + e[7] * acc[1][ct].w));
                    s = red32(pc[0], pc[1]);
                    s = red16(s, s) * __builtin_amdgcn_rcpf(Ls);
                    if (!anynan(s)) break;
                    bool ok = vok(of[0][0]) && vok(of[0][1]) && vok(of[1][0]) && vok(of[1][1]);
#pragma unroll
                    for (int j = 0; j < 8; ++j) ok = ok && le[j] != INV;
                    if (__all(ok)) break;
                    if (layer == 0 && (it & 7) == 7) bump(true);
                    if (it > SPIN_CAP) break;
                }
                CFENCE();
                if (layer == 1) FST(39);
                if ((q & 1) == 0) *(LAS float*)(L + wa) = s;
            }
            BAR();
            if (layer == 1) FST(40);
            STAMP(9 + layer * 12);
            if (layer == 1 && b != 0) { FLUSH(); return; }
            {
                const int d0 = w * 128 + n * 8 + (q >> 1) * 4 + (q & 1) * 2;
                float x1a, x1b;
                const f2 xp = *(LAS f2*)(L + L_XS + d0 * 4);
                int o0 = WS_EA + layer * 16384 + q * 1024 + w * 256 + n * 16;
                PIN(o0);
                nap<D_EA>();
                for (int it = 0;; ++it) {
                    CFENCE();
                    v4u e[4];
#pragma unroll
                    for (int i = 0; i < 4; ++i) e[i] = ld16(o0 + i * 4096);
                    STAMP(10 + layer * 12);
                    float v[8];
#pragma unroll
                    for (int j = 0; j < 4; ++j) {
                        v[2 * j] = dsel(e[3][j], sel_lo, dsel(e[2][j], sel_lo, dsel(e[1][j], sel_lo, bflo(e[0][j]))));
                        v[2 * j + 1] = dsel(e[3][j], sel_hi, dsel(e[2][j], sel_hi, dsel(e[1][j], sel_hi, bfhi(e[0][j]))));
                    }
                    const float z0 = red16(red32(v[0], v[4]), red32(v[2], v[6])), z1 = red16(red32(v[1], v[5]), red32(v[3], v[7]));
                    const unsigned at = pk(z0, z1);
                    const unsigned x1p = pk(xp.x + bflo(at), xp.y + bfhi(at));
                    x1a = bflo(x1p); x1b = bfhi(x1p);
                    if (!anynan(x1a + x1b)) break;
                    if (__all(vok(e[0]) && vok(e[1]) && vok(e[2]) && vok(e[3]))) break;
                    if (layer == 0 && (it & 7) == 7) bump(true);
                    if (it > SPIN_CAP) break;
                }
                CFENCE();
                {
                    const float da = x1a - mu, db = x1b - mu;
                    float s = red32(da + db, da * da + db * db);
                    s = rowsum(red16(s, s));
                    if ((lane & 31) == 0) *(LAS float*)(L + L_ST1 + w * 8 + (lane >> 5) * 4) = s;
                }
                if (layer == 0) FST(42);
                BAR();
                if (layer == 0) FST(43);
                const f4 va = *(LAS f4*)(L + L_ST1), vb = *(LAS f4*)(L + L_ST1 + 16);
                const float S1 = ((va.x + va.z) + (vb.x + vb.z)) * (1.0f / 512.0f), S2 = (va.y + va.w) + (vb.y + vb.w);
                const float mu1 = mu + S1;
                const float var = mx2(fmaf(S2, 1.0f / 512.0f, -S1 * S1), 0.f);
                const float r1 = __builtin_amdgcn_rsqf(var + 1e-5f);
                const float rr = r1 * __builtin_amdgcn_rsqf(fmaf(var * r1, r1, 1e-5f));
                const unsigned hb = pk((x1a - mu1) * rr, (x1b - mu1) * rr);
                const unsigned ta = (hb & 0x7fffu) - (TE0 << 7), tb = ((hb >> 16) & 0x7fffu) - (TE0 << 7);
                const bool ina = ta < 2048u, inb = tb < 2048u;
                const unsigned ia = (ta & 2047u) | ((hb >> 4) & 2048u), ib = (tb & 2047u) | ((hb >> 20) & 2048u);
                float ya = bflo(*(LAS u16*)(L + L_TAB + ia * 2)), yb = bflo(*(LAS u16*)(L + L_TAB + ib * 2));
                if (__any(!(ina && inb))) {
#pragma nounroll
                    for (int half = 0; half < 2; ++half) {
                        unsigned long long miss = __ballot(half ? !inb : !ina);
                        while (miss) {
                            const int src = __builtin_ctzll(miss);
                            miss &= miss - 1;
                            const unsigned hs = (unsigned)__builtin_amdgcn_readlane((int)hb, src);
                            const float h = half ? bfhi(hs) : bflo(hs);
                            f2 ac2 = {0.f, 0.f};
#pragma nounroll
                            for (int f = lane; f < NF; f += 64)
                                ac2 = mixp(f2{h, h}, bflo(*(LAS u16*)(L + L_SO + f * 2)), bflo(*(LAS u16*)(L + L_SO + 4096 + f * 2)), bflo(*(LAS u16*)(L + L_SO + 8192 + f * 2)), ac2);
                            float acc = ac2.x;
                            acc = red32(acc, acc); acc = rowsum(red16(acc, acc));
                            const float yy = bfr(bfr(acc) + b2v);
                            if (lane == src) { if (half) yb = yy; else ya = yy; }
                        }
                    }
                }
                const unsigned x2p = pk(x1a + ya, x1b + yb);
                if (layer == 1) {
                    *(unsigned*)(a.out + d0) = x2p;
                    STAMP(11 + layer * 12); FLUSH();
                    return;
                }
                *(LAS unsigned*)(L + L_XB + d0 * 2) = x2p;
                *(LAS f2*)(L + L_XS + d0 * 4) = f2{bflo(x2p), bfhi(x2p)};
                FST(45);
                BAR();
                FST(46);
                STAMP(11 + layer * 12);
                mu = mu1;
            }
        }
    } else {
        const __amdgpu_buffer_rsrc_t rq = __builtin_amdgcn_make_buffer_rsrc((void*)(a.wq + (size_t)kb * 64 * 4096 + (g * 4 + hq) * 128), 0, 64 * 8192 - 7936, 0x00020000);
        const int ro_ = lane < 32 ? WS_R + g * 512 + lane * 16 : WS_R + 4096 + (lane & 7) * 128;
        v4u kst[8];
#pragma unroll
        for (int i = 0; i < 4; ++i) kst[i] = __builtin_amdgcn_raw_buffer_load_b128(rq, (w3 * 16 + i * 4 + q) * 8192 + n * 16, 0, 2);
        v4u xf[2];
#pragma unroll
        for (int ks = 0; ks < 2; ++ks) xf[ks] = *(const v4u*)(a.x + kb * 64 + ks * 32 + q * 8);
        const v4u x0v = *(const v4u*)(a.x + lane * 8);
        if (w < 7) {
            const __amdgpu_buffer_rsrc_t rp = __builtin_amdgcn_make_buffer_rsrc((void*)(w == 4 ? a.w1 : w == 5 ? a.b1 : a.w2), 0, NF * 2, 0x00020000);
#pragma unroll
            for (int i = 0; i < 4; ++i) kst[4 + i] = __builtin_amdgcn_raw_buffer_load_b128(rp, i * 1024 + lane * 16, 0, 0);
        } else {
            const v4u iv = {INV, INV, INV, INV};
            st16(WS_EB + (lane >> 5) * 131072 + g * 16384 + kb * 2048 + hq * 512 + (lane & 31) * 16, iv);
            int o;
            if (lane < 32) o = WS_EB + 2 * 131072 + g * 16384 + kb * 2048 + hq * 512 + lane * 16;
            else if (lane < 40) o = WS_EA + ((lane >> 2) & 1) * 16384 + (g * 2 + hp) * 1024 + cb * 64 + (lane & 3) * 16;
            else if (lane < 48) o = WS_EL + ((lane >> 2) & 1) * 65536 + g * 8192 + (lane & 3) * 2048 + r * 64;
            else o = WS_TAB + b * 32 + (lane & 1) * 16;
            if (lane < 50) st16(o, iv);
            st16(WS_EC + g * 32768 + (lane >> 2) * 2048 + r * 64 + (lane & 3) * 16, iv);
            st16(WS_EC + 262144 + g * 32768 + (lane >> 2) * 2048 + r * 64 + (lane & 3) * 16, iv);
        }
        const float b2v = bflo(a.b2[0]);
        BAR();
#pragma unroll
        for (int i = 0; i < 4; ++i) { const int row = w3 * 16 + i * 4 + q;
            *(LAS v4u*)(L + L_SQ + row * 256 + ((n ^ (((row & 3) << 2) | ((row >> 2) & 3))) * 16)) = kst[i]; }
        if (w < 7) {
#pragma unroll
            for (int i = 0; i < 4; ++i) *(LAS v4u*)(L + L_SO + w3 * 4096 + i * 1024 + lane * 16) = kst[4 + i];
        } else {
            asm volatile("s_waitcnt vmcnt(0)" ::: "memory");
            const v2u t = __builtin_bit_cast(v2u, (unsigned long long)__builtin_amdgcn_s_memrealtime());
            if (lane == 0) st16(WS_R + g * 512 + r * 16, v4u{t.x, t.y, t.x, t.y});
        }
        if (n == 1) { xf[0] = v4u{0x3f803f80u, 0x3f803f80u, 0x3f803f80u, 0x3f803f80u}; xf[1] = xf[0]; }
        asm volatile("" :: "v"(cpf), "v"(cpf2));
        STAMP(1);
        asm volatile("s_waitcnt lgkmcnt(0)" ::: "memory");
        if (lane == 0) __hip_atomic_fetch_add((LAS unsigned*)(L + L_GATE), 1u, __ATOMIC_RELAXED, __HIP_MEMORY_SCOPE_WORKGROUP);
        while (*gate < 4u) __builtin_amdgcn_s_sleep(1);
        CFENCE();
        v4u wqf[2][2];
        {
            const int aa = n >> 2, p = n & 3;
#pragma unroll
            for (int ct = 0; ct < 2; ++ct)
#pragma unroll
                for (int ks = 0; ks < 2; ++ks) {
                    v2u t[2];
#pragma unroll
                    for (int tt = 0; tt < 2; ++tt) { const int row = ks * 32 + q * 8 + tt * 4 + aa, ch = 4 * w3 + 2 * ct + (p >> 1);
                        t[tt] = trd(L + L_SQ + row * 256 + ((ch ^ (((row & 3) << 2) | ((row >> 2) & 3))) * 16) + 8 * (p & 1)); }
                    wqf[ct][ks] = v4u{t[0].x, t[0].y, t[1].x, t[1].y};
                }
#pragma unroll
            for (int ct = 0; ct < 2; ++ct) {
                const f4 c = mfma32(wqf[ct][1], xf[1], mfma32(wqf[ct][0], xf[0], f4{0, 0, 0, 0}));
                if (n < 2) st16(WS_EB + n * 2 * 131072 + g * 16384 + kb * 2048 + hq * 512 + w3 * 128 + ct * 64 + q * 16, __builtin_bit_cast(v4u, c));
            }
        }
        SB();
        STAMP(2);
        f2 csum = {0.f, 0.f};
        auto fetch_q = [&](int layer, auto&& meanwhile) {
            int o0 = WS_EB + layer * 131072 + g * 16384 + (lane >> 5) * 2048 + w3 * 512 + (lane & 31) * 16;
            int qa = L_QS + (w3 * 128 + (lane & 31) * 4 + (lane >> 5) * 2) * 2;
            PIN(o0); PIN(qa);
            f2 qv;
            if (layer == 1) nap<D_EB>();
            bool ready = layer == 1;
            for (int it = 0;; ++it) {
                CFENCE();
                v4u e[4], c[4], rec = {0, 0, 0, 0};
                unsigned long long lim = 0;
                if (layer == 0) { lim = __builtin_amdgcn_s_memrealtime() - MARGIN; SB(); if (!ready && lane < 32) rec = ld16(ro_); }
#pragma unroll
                for (int i = 0; i < 4; ++i) e[i] = ld16(o0 + i * 4096);
                if (layer == 0) {
#pragma unroll
                    for (int i = 0; i < 4; ++i) c[i] = ld16(o0 + 2 * 131072 + i * 4096);
                    SB();
                    if (it == 0) meanwhile();
                    SB();
                    const f4 cs = (__builtin_bit_cast(f4, c[0]) + __builtin_bit_cast(f4, c[1])) + (__builtin_bit_cast(f4, c[2]) + __builtin_bit_cast(f4, c[3]));
                    csum = f2{red32(cs.x, cs.z), red32(cs.y, cs.w)};
                    if (!ready) ready = __all(lane >= 32 || within(rec.x, rec.y, lim) || within(rec.z, rec.w, lim));
                }
                const f4 es = (__builtin_bit_cast(f4, e[0]) + __builtin_bit_cast(f4, e[1])) + (__builtin_bit_cast(f4, e[2]) + __builtin_bit_cast(f4, e[3]));
                qv = (f2{red32(es.x, es.z), red32(es.y, es.w)} - csum * mu) * rsig;
                if (ready) {
                    if (!anynan(qv.x + qv.y)) break;
                    bool ok = vok(e[0]) && vok(e[1]) && vok(e[2]) && vok(e[3]);
                    if (layer == 0) ok = ok && vok(c[0]) && vok(c[1]) && vok(c[2]) && vok(c[3]);
                    if (__all(ok)) break;
                }
                if (layer == 0 && (it & 7) == 7) bump(false);
                if (it > SPIN_CAP) break;
            }
            CFENCE();
            *(LAS unsigned*)(L + qa) = pk(qv.x, qv.y);
        };
        float w1f[7], b1f[7], w2f[7];
            f2 tacc[8];
            const unsigned hbits0 = (unsigned)((b >> 7) << 15) | (unsigned)((TE0 << 7) + (b & 127) * 16);
            auto tab_pair = [&](int e) {
                const f2 h = {u2f((hbits0 + e) << 16), u2f((hbits0 + e + 8) << 16)};
                f2 acc = {0.f, 0.f};
#pragma unroll
                for (int j = 0; j < 7; ++j) acc = mixp(h, w1f[j], b1f[j], w2f[j], acc);
                asm volatile("" : "+v"(acc));
                tacc[e] = acc;
            };
#pragma unroll
            for (int j = 0; j < 7; ++j) { const int f = ((tid & 255) + 256 * j) * 2;
                w1f[j] = bflo(*(LAS u16*)(L + L_SO + f)); b1f[j] = bflo(*(LAS u16*)(L + L_SO + 4096 + f)); w2f[j] = bflo(*(LAS u16*)(L + L_SO + 8192 + f)); }
            ln_stats(f4{bflo(x0v.x), bfhi(x0v.x), bflo(x0v.y), bfhi(x0v.y)}, f4{bflo(x0v.z), bfhi(x0v.z), bflo(x0v.w), bfhi(x0v.w)});
            SB();
#pragma unroll
            for (int e = 0; e < TG; ++e) tab_pair(e);
            SB();
            STAMP(3);
            fetch_q(0, [&]() {
#pragma unroll
                for (int e = TG; e < TA; ++e) tab_pair(e);
            });
            if (w == 7) bump(true);
            STAMP(4);
            BAR();
#pragma unroll
            for (int e = TA; e < TB2; ++e) tab_pair(e);
            STAMP(5);
#pragma unroll
        for (int layer = 0; layer < 2; ++layer) {
            if (layer == 1) BAR();
            int ea = WS_EC + layer * 262144 + g * 32768 + (w3 >> 1) * 16384 + ((w3 & 1) * 4 + q) * 2048 + r * 64 + (n >> 2) * 16;
            int oa = L_OW + w3 * 528 + lane * 8;
            PIN(ea); PIN(oa);
            BAR();
            if (layer == 1) FST(36);
            {
                const f4 ma = *(LAS f4*)(L + L_M + w3 * 32), la = *(LAS f4*)(L + L_M + w3 * 32 + 16);
                f2 v[4];
#pragma unroll
                for (int ww = 0; ww < 4; ++ww) v[ww] = *(LAS f2*)(L + oa + ww * 2112);
                const float M = mx2(mx3(ma.x, ma.y, ma.z), ma.w);
                const float e0 = __builtin_amdgcn_exp2f(ma.x - M), e1 = __builtin_amdgcn_exp2f(ma.y - M), e2 = __builtin_amdgcn_exp2f(ma.z - M), e3 = __builtin_amdgcn_exp2f(ma.w - M);
                const float Ls = (e0 * la.x + e1 * la.y) + (e2 * la.z + e3 * la.w);
                const f2 oo = (v[0] * e0 + v[1] * e1) + (v[2] * e2 + v[3] * e3);
                const float inv = __builtin_amdgcn_rcpf(Ls);
                const unsigned wd = nn(pk(oo.x * inv, oo.y * inv));
                const v4u vv = {wd, dppu<0x55>(wd), dppu<0xAA>(wd), dppu<0xFF>(wd)};
                if ((lane & 3) == 0) st16(ea, vv);
                if (lane == 0) st4(WS_EL + layer * 65536 + g * 8192 + w3 * 2048 + r * 64, f2u(M + __builtin_amdgcn_logf(Ls)));
            }
            STAMP(7 + layer * 12);
            if (layer == 0) {
#pragma unroll
                for (int e = TB2; e < 8; ++e) tab_pair(e);
                float t8[8], t4[4];
#pragma unroll
                for (int j = 0; j < 8; ++j) t8[j] = red32(tacc[j].x, tacc[j].y);
#pragma unroll
                for (int j = 0; j < 4; ++j) t4[j] = rowsum(red16(t8[j], t8[j + 4]));
                if (n == 0) *(LAS f4*)(L + L_TR + w3 * 64 + q * 16) = f4{t4[0], t4[1], t4[2], t4[3]};
                STAMP(6);
                if (w == 7) {
                    for (int it = 0;; ++it) {
                        CFENCE();
                        v4u cur = {0, 0, 0, 0};
                        if (lane >= 32) cur = ld16(ro_);
                        if (__all(lane < 32 || within(cur.x, cur.y, __builtin_amdgcn_s_memrealtime()))) break;
                        if ((it & 3) == 3) bump(true);
                        if (it > SPIN_CAP) break;
                        nap<16>();
                    }
                    CFENCE();
                }
            }
            BAR();
            if (w == 4 && lane < 16) {
                f2 v[4];
#pragma unroll
                for (int ww = 0; ww < 4; ++ww) v[ww] = *(LAS f2*)(L + L_WR + ww * 128 + lane * 8);
                const f2 sv = (v[0] + v[1]) + (v[2] + v[3]);
                const unsigned wd = nn(pk(sv.x, sv.y));
                const v4u vv = {wd, dppu<0x55>(wd), dppu<0xAA>(wd), dppu<0xFF>(wd)};
                if ((lane & 3) == 0) st16(WS_EA + layer * 16384 + (g * 2 + hp) * 1024 + cb * 64 + lane * 4, vv);
            }
            if (layer == 0 && w == 5 && lane < 8) {
                f2 tv[4];
#pragma unroll
                for (int ww = 0; ww < 4; ++ww) tv[ww] = *(LAS f2*)(L + L_TR + ww * 64 + lane * 8);
                const f2 sv = (tv[0] + tv[1]) + (tv[2] + tv[3]);
                const unsigned wd = nn(pk(bfr(sv.x) + b2v, bfr(sv.y) + b2v));
                const v4u vv = {wd, dppu<0x55>(wd), dppu<0xAA>(wd), dppu<0xFF>(wd)};
                if ((lane & 3) == 0) st16(WS_TAB + b * 32 + lane * 4, vv);
            }
            STAMP(9 + layer * 12);
            if (layer == 1) { FLUSH(); return; }
            nap<D_EA>();
            for (int it = 0;; ++it) {
                CFENCE();
                const v4u t0 = ld16(WS_TAB + w3 * 2048 + lane * 16), t1 = ld16(WS_TAB + w3 * 2048 + 1024 + lane * 16);
                *(LAS v4u*)(L + L_TAB + w3 * 2048 + lane * 16) = t0; *(LAS v4u*)(L + L_TAB + w3 * 2048 + 1024 + lane * 16) = t1;
                if (__all(vok(t0) && vok(t1))) break;
                if ((it & 7) == 7) bump(true);
                if (it > SPIN_CAP) break;
            }
            CFENCE();
            int qo = WS_EB + 131072 + g * 16384 + kb * 2048 + hq * 512 + w3 * 128 + n * 64 + q * 16;
            int xa_ = L_XB + kb * 128 + q * 16;
            PIN(qo); PIN(xa_);
            BAR();
            BAR();
            {
                const v4u xa = *(LAS v4u*)(L + xa_), xb = *(LAS v4u*)(L + xa_ + 64);
                const f4 c0 = mfma32(wqf[0][1], xb, mfma32(wqf[0][0], xa, f4{0, 0, 0, 0})), c1 = mfma32(wqf[1][1], xb, mfma32(wqf[1][0], xa, f4{0, 0, 0, 0}));
                if (n < 2) st16(qo, __builtin_bit_cast(v4u, n == 0 ? c0 : c1));
            }
            STAMP(11 + layer * 12);
            SB();
            ln_stats(*(LAS f4*)(L + L_XS + lane * 32), *(LAS f4*)(L + L_XS + lane * 32 + 16));
            fetch_q(1, []() {});
            STAMP(16);
        }
    }
}

}

extern "C" void kernel_launch(void* const* d_in, const int* in_sizes, int n_in,
                              void* d_out, int out_size, void* d_ws, size_t ws_size,
                              hipStream_t stream) {
    fz::Args a;
    a.x = (const u16*)d_in[0]; a.kc = (const u16*)d_in[1]; a.vc = (const u16*)d_in[2];
    a.wq = (const u16*)d_in[3]; a.wo = (const u16*)d_in[4];
    a.w1 = (const u16*)d_in[6]; a.b1 = (const u16*)d_in[7]; a.w2 = (const u16*)d_in[8]; a.b2 = (const u16*)d_in[9];
    a.out = (u16*)d_out; a.ws = (unsigned char*)d_ws;
    (void)hipFuncSetAttribute((const void*)fz::fused, hipFuncAttributeMaxDynamicSharedMemorySize, fz::LDS_BYTES);
    fz::fused<<<256, 512, fz::LDS_BYTES, stream>>>(a);
}
```

```cpp
#include <hip/hip_runtime.h>
#include <hip/hip_bf16.h>
#include <stdint.h>

typedef unsigned short u16;
typedef unsigned v4u __attribute__((ext_vector_type(4)));
typedef unsigned v2u __attribute__((ext_vector_type(2)));
typedef float f4 __attribute__((ext_vector_type(4)));
typedef float f2 __attribute__((ext_vector_type(2)));
typedef short s4 __attribute__((ext_vector_type(4)));
typedef __bf16 b8 __attribute__((ext_vector_type(8)));
typedef __bf16 b2 __attribute__((ext_vector_type(2)));
#define LAS __attribute__((address_space(3)))
#define GAS __attribute__((address_space(1)))

#define D_EB 8
#define D_EC 24
#define D_EA 20
#define CODE_PF 16384
#define TA 3
#define TB 12
#define D_K 8
#define D_EC0 4
#define TG0 2
#define TG 1
#define VH 4
#define WN 4
#define TB2 5
#define STAMPS 0

namespace fz {

constexpr int SK = 65536, WIN = 4096, NF = 1792;
constexpr unsigned INV = 0xFFFFFFFFu;
constexpr int WS_R = 0;
constexpr int WS_TAB = 8192;
constexpr int WS_EB = 16384;
constexpr int WS_EC = WS_EB + 3 * 131072;
constexpr int WS_EL = WS_EC + 2 * 262144;
constexpr int WS_EA = WS_EL + 2 * 65536;
constexpr int WS_END = WS_EA + 2 * 16384;
constexpr int WS_STAMP = 2 << 20;
constexpr int L_STG = 0;
constexpr int L_SQ = 32768;
constexpr int L_SO = L_SQ + 16384;
constexpr int L_TAB = L_SO + 16384;
constexpr int L_OW = L_TAB + 8192;
constexpr int L_QS = L_OW + 4 * 4 * 132 * 4;
constexpr int L_XB = L_QS + 1024;
constexpr int L_XS = L_XB + 1024;
constexpr int L_M = L_XS + 2048;
constexpr int L_WR = L_M + 128;
constexpr int L_ST1 = L_WR + 512;
constexpr int L_ST2 = L_ST1 + 64;
constexpr int L_TR = L_ST2 + 64;
constexpr int L_GATE = L_TR + 512;
constexpr int L_END = L_GATE + 16;
constexpr int LDS_BYTES = 100 * 1024;
static_assert(L_END <= LDS_BYTES, "lds");
constexpr int TE0 = 115;
constexpr int SPIN_CAP = 1 << 22;
constexpr unsigned long long MARGIN = 20;

__device__ __forceinline__ float u2f(unsigned u) { return __builtin_bit_cast(float, u); }
__device__ __forceinline__ unsigned f2u(float f) { return __builtin_bit_cast(unsigned, f); }
__device__ __forceinline__ float bflo(unsigned u) { return u2f(u << 16); }
__device__ __forceinline__ float bfhi(unsigned u) { return u2f(u & 0xffff0000u); }
__device__ __forceinline__ float bfr(float x) { return (float)(__bf16)x; }
__device__ __forceinline__ unsigned pk(float lo, float hi) { b2 v = {(__bf16)lo, (__bf16)hi}; return __builtin_bit_cast(unsigned, v); }
__device__ __forceinline__ unsigned nn(unsigned w) { return w == INV ? 0x7fc07fc0u : w; }
__device__ __forceinline__ f4 mfma32(v4u a, v4u b, f4 c) { return __builtin_amdgcn_mfma_f32_16x16x32_bf16(__builtin_bit_cast(b8, a), __builtin_bit_cast(b8, b), c, 0, 0, 0); }
__device__ __forceinline__ f4 mfma16(v2u a, v2u b, f4 c) { return __builtin_amdgcn_mfma_f32_16x16x16bf16_1k(__builtin_bit_cast(s4, a), __builtin_bit_cast(s4, b), c, 0, 0, 0); }
__device__ __forceinline__ v2u trd(LAS unsigned char* p) { return __builtin_bit_cast(v2u, __builtin_amdgcn_ds_read_tr16_b64_v4i16((LAS s4*)p)); }
template <int CTRL> __device__ __forceinline__ float dpp(float x) { return u2f(__builtin_amdgcn_mov_dpp(f2u(x), CTRL, 0xf, 0xf, true)); }
template <int CTRL> __device__ __forceinline__ unsigned dppu(unsigned x) { return (unsigned)__builtin_amdgcn_mov_dpp((int)x, CTRL, 0xf, 0xf, true); }
constexpr int ROR8 = 0x128, ROR4 = 0x124, ROR2 = 0x122, ROR1 = 0x121;
__device__ __forceinline__ float red32(float a, float b) { auto r = __builtin_amdgcn_permlane32_swap(f2u(a), f2u(b), false, false); return u2f(r[0]) + u2f(r[1]); }
__device__ __forceinline__ float red16(float a, float b) { auto r = __builtin_amdgcn_permlane16_swap(f2u(a), f2u(b), false, false); return u2f(r[0]) + u2f(r[1]); }
__device__ __forceinline__ float mx2(float a, float b) { return fmaxf(a, b); }
__device__ __forceinline__ float mx3(float a, float b, float c) { return fmaxf(fmaxf(a, b), c); }
__device__ __forceinline__ float max32(float a) { auto r = __builtin_amdgcn_permlane32_swap(f2u(a), f2u(a), false, false); return mx2(u2f(r[0]), u2f(r[1])); }
__device__ __forceinline__ float max16(float a) { auto r = __builtin_amdgcn_permlane16_swap(f2u(a), f2u(a), false, false); return mx2(u2f(r[0]), u2f(r[1])); }
__device__ __forceinline__ float dsel(unsigned p, unsigned sel, float acc) { return __builtin_amdgcn_fdot2_f32_bf16(__builtin_bit_cast(b2, p), __builtin_bit_cast(b2, sel), acc, false); }
__device__ __forceinline__ f2 mixp(f2 h, float w1, float b1, float w2, f2 acc) {
    const float c0 = -2.0f * 0.7978845608028654f * 1.4426950408889634f, c1 = c0 * 0.044715f;
    const f2 u = h * w1 + b1;
    const f2 z = u * (u * u * c1 + c0);
    const f2 d = f2{__builtin_amdgcn_exp2f(z.x), __builtin_amdgcn_exp2f(z.y)} + 1.0f;
    const f2 gq = u * f2{__builtin_amdgcn_rcpf(d.x), __builtin_amdgcn_rcpf(d.y)};
    return gq * w2 + acc;
}
__device__ __forceinline__ bool anynan(float x) { return __any(x != x); }
#define PIN(x) asm volatile("" : "+v"(x))
__device__ __forceinline__ float rowsum(float x) { x += dpp<ROR8>(x); x += dpp<ROR4>(x); x += dpp<ROR2>(x); x += dpp<ROR1>(x); return x; }
#define BAR() asm volatile("s_waitcnt lgkmcnt(0)\n\ts_barrier" ::: "memory")
#define CFENCE() asm volatile("" ::: "memory")
#define SB() __builtin_amdgcn_sched_barrier(0)
template <int N> __device__ __forceinline__ void nap() {
#pragma unroll
    for (int i = 0; i < N / 8; ++i) __builtin_amdgcn_s_sleep(8);
    if (N % 8) __builtin_amdgcn_s_sleep(N % 8);
}
__device__ __forceinline__ bool vok(v4u a) { return a.x != INV && a.y != INV && a.z != INV && a.w != INV; }

__device__ __forceinline__ float gelu(float u) {
    const float c0 = -2.0f * 0.7978845608028654f * 1.4426950408889634f, c1 = c0 * 0.044715f;
    float e = __builtin_amdgcn_exp2f(u * fmaf(u * u, c1, c0));
    return u * __builtin_amdgcn_rcpf(1.0f + e);
}
__device__ __forceinline__ float mix2(float h, float w1a, float w1b, float b1a, float b1b, unsigned w2p, float acc) {
    unsigned m = pk(h * w1a, h * w1b);
    unsigned u = pk(bflo(m) + b1a, bfhi(m) + b1b);
    unsigned gq = pk(gelu(bflo(u)), gelu(bfhi(u)));
    return __builtin_amdgcn_fdot2_f32_bf16(__builtin_bit_cast(b2, gq), __builtin_bit_cast(b2, w2p), acc, false);
}

struct Args {
    const u16 *x, *kc, *vc, *wq, *wo, *w1, *b1, *w2, *b2;
    u16* out;
    unsigned char* ws;
};

#define FST(i) do {} while (0)
#define FLUSH() do {} while (0)
#define STAMP(i) do {} while (0)

__global__ void __launch_bounds__(512, 2) fused(Args a) {
    extern __shared__ __attribute__((aligned(16))) unsigned char smem[];
    LAS unsigned char* L = (LAS unsigned char*)smem;
    const int b = blockIdx.x, tid = threadIdx.x;
    v4u cpf, cpf2;
    {
        unsigned long long pc;
        unsigned co, co2;
        asm volatile("s_getpc_b64 %4\n\tv_lshlrev_b32 %2, 4, %5\n\tv_min_u32 %3, %6, %2\n\tv_add_u32 %3, %7, %3\n\tglobal_load_dwordx4 %0, %2, %4\n\tglobal_load_dwordx4 %1, %3, %4"
                     : "=&v"(cpf), "=&v"(cpf2), "=&v"(co), "=&v"(co2), "=&s"(pc) : "v"(tid), "i"(CODE_PF - 8192 - 16), "i"(8192) : "memory");
        SB();
    }
    const int w = __builtin_amdgcn_readfirstlane(tid >> 6), lane = tid & 63;
    const int w3 = w & 3;
    const int g = b & 7, r = b >> 3;
    const int kb = r & 7, hq = r >> 3, hp = r >> 4, cb = r & 15;
    const int n = lane & 15, q = lane >> 4;
    const __amdgpu_buffer_rsrc_t rs = __builtin_amdgcn_make_buffer_rsrc((void*)a.ws, 0, WS_END, 0x00020000);
    auto ld16 = [&](int off) -> v4u { return __builtin_amdgcn_raw_buffer_load_b128(rs, off, 0, 16); };
    auto ld4 = [&](int off) -> unsigned { return __builtin_amdgcn_raw_buffer_load_b32(rs, off, 0, 16); };
    auto st16 = [&](int off, v4u v) { __builtin_amdgcn_raw_buffer_store_b128(v, rs, off, 0, 16); };
    auto st4 = [&](int off, unsigned v) { __builtin_amdgcn_raw_buffer_store_b32(v, rs, off, 0, 16); };
    const unsigned long long t_start = __builtin_amdgcn_s_memrealtime();
    auto bump = [&](bool tok) {
        const unsigned long long t = __builtin_amdgcn_s_memrealtime();
        if (lane == 0 || (lane == 1 && r == 0 && tok))
            __builtin_amdgcn_raw_buffer_store_b64(__builtin_bit_cast(v2u, t), rs, lane == 0 ? WS_R + g * 512 + r * 16 + 8 : WS_R + 4096 + g * 128, 0, 16);
    };
    auto within = [&](unsigned lo, unsigned hi, unsigned long long lim) -> bool { return (((unsigned long long)hi << 32) | lo) - t_start <= lim - t_start; };
    volatile LAS unsigned* gate = (volatile LAS unsigned*)(L + L_GATE);
    int vst = 0; (void)vst;
    STAMP(0);
    if (w == 0) *(LAS v4u*)(L + L_GATE) = v4u{0u, 0u, 0u, 0u};

    float mu, rsig;
    auto ln_stats = [&](f4 xa, f4 xb) {
        float s = ((xa.x + xa.y) + (xa.z + xa.w)) + ((xb.x + xb.y) + (xb.z + xb.w));
        s = red32(s, s); s = red16(s, s); s = rowsum(s);
        mu = s * (1.0f / 512.0f);
        const f4 da = xa - mu, db = xb - mu;
        float v = ((da.x * da.x + da.y * da.y) + (da.z * da.z + da.w * da.w)) + ((db.x * db.x + db.y * db.y) + (db.z * db.z + db.w * db.w));
        v = red32(v, v); v = red16(v, v); v = rowsum(v);
        rsig = __builtin_amdgcn_rsqf(v * (1.0f / 512.0f) + 1e-5f);
    };

    if (w < 4) {
        const size_t kvb = ((size_t)g * SK + (SK - WIN) + r * 128 + w * 4) * 128;
        const __amdgpu_buffer_rsrc_t rk = __builtin_amdgcn_make_buffer_rsrc((void*)(a.kc + kvb), 0, 32768 - 3072, 0x00020000);
        const __amdgpu_buffer_rsrc_t rv = __builtin_amdgcn_make_buffer_rsrc((void*)(a.vc + kvb), 0, 32768 - 3072, 0x00020000);
        const __amdgpu_buffer_rsrc_t ro = __builtin_amdgcn_make_buffer_rsrc((void*)(a.wo + ((size_t)(g * 2 + hp) * 256 + w * 64) * 512 + cb * 32), 0, 64 * 1024 - 960, 0x00020000);
        unsigned sel_lo = 0x00003f80u, sel_hi = 0x3f800000u;
        asm volatile("" : "+s"(sel_lo), "+s"(sel_hi));
        const unsigned mk0 = (n >> 2) == 0 ? ~0u : 0u, mk1 = (n >> 2) == 1 ? ~0u : 0u, mk2 = (n >> 2) == 2 ? ~0u : 0u, mk3 = (n >> 2) == 3 ? ~0u : 0u;
        LAS unsigned char* stg = L + L_STG + w * 8192;
        const float b2v = bflo(a.b2[0]);
        const v4u x0v = *(const v4u*)(a.x + lane * 8);
        BAR();
        nap<D_K>();
        v4u sb[20];
        auto issue = [&](int j) {
            if (j < 8) sb[j] = __builtin_amdgcn_raw_buffer_load_b128(rk, j * 4096 + lane * 16, 0, 2);
            else if (j < 16) sb[j] = __builtin_amdgcn_raw_buffer_load_b128(rv, (j - 8) * 4096 + lane * 16, 0, 2);
            else if (j < 20) sb[j] = __builtin_amdgcn_raw_buffer_load_b128(ro, ((j - 16) * 16 + (lane >> 2)) * 1024 + (lane & 3) * 16, 0, 2);
            SB();
        };
#pragma unroll
        for (int j = 0; j < WN; ++j) issue(j);
        {
            const f4 xa = {bflo(x0v.x), bfhi(x0v.x), bflo(x0v.y), bfhi(x0v.y)}, xb = {bflo(x0v.z), bfhi(x0v.z), bflo(x0v.w), bfhi(x0v.w)};
            ln_stats(xa, xb);
            if (w == 2) { *(LAS f4*)(L + L_XS + lane * 32) = xa; *(LAS f4*)(L + L_XS + lane * 32 + 16) = xb; }
        }
        asm volatile("" :: "v"(cpf), "v"(cpf2));
        STAMP(1);
#pragma unroll
        for (int i = 0; i < 8; ++i) {
            const int row = i * 4 + q; *(LAS v4u*)(stg + row * 256 + ((n ^ (row & 15)) * 16)) = sb[i];
            SB();
            issue(i + WN);
        }
        STAMP(3);
        v4u kf[2][4], vf[8], wof[2][2];
#pragma unroll
        for (int t = 0; t < 2; ++t)
#pragma unroll
            for (int ks = 0; ks < 4; ++ks) kf[t][ks] = *(LAS v4u*)(stg + (t * 16 + n) * 256 + (((ks * 4 + q) ^ n) * 16));
        SB();
#pragma unroll
        for (int i = 0; i < VH; ++i) {
            const int row = i * 4 + q; *(LAS v4u*)(stg + row * 256 + ((n ^ ((row & 7) << 1)) * 16)) = sb[8 + i];
            SB();
            issue(8 + i + WN);
        }

#pragma unroll
        for (int layer = 0; layer < 2; ++layer) {
            STAMP(4 + layer * 12);
            BAR();
            if (layer == 1) FST(32);
            {
                v4u qf[4];
#pragma unroll
                for (int ks = 0; ks < 4; ++ks) qf[ks] = *(LAS v4u*)(L + L_QS + (n & 3) * 256 + ks * 64 + q * 16);
                SB();
                f4 s0 = {0, 0, 0, 0}, s1 = {0, 0, 0, 0};
#pragma unroll
                for (int ks = 0; ks < 4; ++ks) { s0 = mfma32(kf[0][ks], qf[ks], s0); s1 = mfma32(kf[1][ks], qf[ks], s1); }
                const float C2 = 0.08838834764831845f * 1.4426950408889634f;
                s0 *= C2; s1 *= C2;
                float m = fmaxf(fmaxf(fmaxf(s0.x, s0.y), fmaxf(s0.z, s0.w)), fmaxf(fmaxf(s1.x, s1.y), fmaxf(s1.z, s1.w)));
                m = max32(max16(m));
                float e[8];
                e[0] = __builtin_amdgcn_exp2f(s0.x - m); e[1] = __builtin_amdgcn_exp2f(s0.y - m);
                e[2] = __builtin_amdgcn_exp2f(s0.z - m); e[3] = __builtin_amdgcn_exp2f(s0.w - m);
                e[4] = __builtin_amdgcn_exp2f(s1.x - m); e[5] = __builtin_amdgcn_exp2f(s1.y - m);
                e[6] = __builtin_amdgcn_exp2f(s1.z - m); e[7] = __builtin_amdgcn_exp2f(s1.w - m);
                const v4u pf = {pk(e[0], e[1]), pk(e[2], e[3]), pk(e[4], e[5]), pk(e[6], e[7])};
                STAMP(5 + layer * 12);
                if (layer == 0) {
#pragma unroll
                    for (int i = VH; i < 8; ++i) {
                        const int row = i * 4 + q; *(LAS v4u*)(stg + row * 256 + ((n ^ ((row & 7) << 1)) * 16)) = sb[8 + i];
                        SB();
                        issue(8 + i + WN);
                    }
                    const int row = q * 4 + (n >> 2), p = n & 3;
#pragma unroll
                    for (int c = 0; c < 8; ++c) {
                        LAS unsigned char* ap = stg + row * 256 + (((2 * c + (p >> 1)) ^ ((row & 7) << 1)) * 16) + 8 * (p & 1);
                        const v2u t0 = trd(ap), t1 = trd(ap + 4096);
                        vf[c] = v4u{t0.x, t0.y, t1.x, t1.y};
                    }
                }
                STAMP(6 + layer * 12);
                f4 oa = {0, 0, 0, 0}, ob = {0, 0, 0, 0};
                { const v4u pm = pf & mk0; oa = mfma32(vf[0], pm, oa); ob = mfma32(vf[4], pm, ob); }
                { const v4u pm = pf & mk1; oa = mfma32(vf[1], pm, oa); ob = mfma32(vf[5], pm, ob); }
                { const v4u pm = pf & mk2; oa = mfma32(vf[2], pm, oa); ob = mfma32(vf[6], pm, ob); }
                { const v4u pm = pf & mk3; oa = mfma32(vf[3], pm, oa); ob = mfma32(vf[7], pm, ob); }
                float ls = ((e[0] + e[1]) + (e[2] + e[3])) + ((e[4] + e[5]) + (e[6] + e[7]));
                ls = red32(ls, ls); ls = red16(ls, ls);
                if (lane < 4) { *(LAS float*)(L + L_M + n * 32 + w * 4) = m; *(LAS float*)(L + L_M + n * 32 + 16 + w * 4) = ls; }
                *(LAS f4*)(L + L_OW + (w * 4 + (n & 3)) * 528 + (n >> 2) * 64 + q * 16) = oa;
                *(LAS f4*)(L + L_OW + (w * 4 + (n & 3)) * 528 + (n >> 2) * 64 + 256 + q * 16) = ob;
            }
            if (layer == 1) FST(35);
            BAR();
            if (layer == 1) FST(36);
            {
                int o0 = WS_EC + layer * 262144 + g * 32768 + hp * 16384 + w * 4096 + n * 64 + q * 16;
                int l0 = WS_EL + layer * 65536 + g * 8192 + (hp * 2 + (w >> 1)) * 2048 + q * 256;
                int wa = L_WR + w * 128 + (q >> 1) * 64 + n * 4;
                PIN(o0); PIN(l0); PIN(wa);
                if (layer == 0) {
#pragma unroll
                    for (int i = 0; i < 4; ++i) { const int row = i * 16 + (lane >> 2);
                        *(LAS v4u*)(stg + row * 64 + (((lane & 3) ^ (((row >> 3) & 1) << 1)) * 16)) = sb[16 + i]; }
                    const int aa = n >> 2, p = n & 3;
#pragma unroll
                    for (int ks = 0; ks < 2; ++ks)
#pragma unroll
                        for (int ct = 0; ct < 2; ++ct) {
                            v2u t[2];
#pragma unroll
                            for (int tt = 0; tt < 2; ++tt) { const int row = ks * 32 + q * 8 + tt * 4 + aa;
                                t[tt] = trd(stg + row * 64 + (((2 * ct + (p >> 1)) ^ (((row >> 3) & 1) << 1)) * 16) + 8 * (p & 1)); }
                            wof[ks][ct] = v4u{t[0].x, t[0].y, t[1].x, t[1].y};
                        }
                    nap<D_EC0>();
                } else nap<D_EC>();
                float s;
                for (int it = 0;; ++it) {
                    if (layer == 0 || it > 0) {
                        for (int i2 = 0;; ++i2) {
                            CFENCE();
                            const unsigned f0 = ld4(l0 - q * 256 + (lane & 31) * 64);
                            if (__all(f0 != INV)) break;
                            if (layer == 0 && (i2 & 7) == 7) bump(true);
                            if (i2 > SPIN_CAP) break;
                            nap<4>();
                        }
                    }
                    CFENCE();
                    v4u of[2][2];
                    unsigned le[8];
#pragma unroll
                    for (int j = 0; j < 8; ++j) le[j] = ld4(l0 + (j >> 2) * 1024 + (j & 3) * 64);
#pragma unroll
                    for (int ks = 0; ks < 2; ++ks)
#pragma unroll
                        for (int st = 0; st < 2; ++st) of[ks][st] = ld16(o0 + ks * 2048 + st * 1024);
                    STAMP(8 + layer * 12);
                    float M = mx3(mx3(u2f(le[0]), u2f(le[1]), u2f(le[2])), mx3(u2f(le[3]), u2f(le[4]), u2f(le[5])), mx2(u2f(le[6]), u2f(le[7])));
                    M = max32(max16(M));
                    float e[8];
#pragma unroll
                    for (int j = 0; j < 8; ++j) e[j] = __builtin_amdgcn_exp2f(u2f(le[j]) - M);
                    float Ls = ((e[0] + e[1]) + (e[2] + e[3])) + ((e[4] + e[5]) + (e[6] + e[7]));
                    Ls = red32(Ls, Ls); Ls = red16(Ls, Ls);
                    f4 acc[2][2];
#pragma unroll
                    for (int st = 0; st < 2; ++st)
#pragma unroll
                        for (int ct = 0; ct < 2; ++ct) acc[st][ct] = mfma32(of[1][st], wof[1][ct], mfma32(of[0][st], wof[0][ct], f4{0, 0, 0, 0}));
                    float pc[2];
#pragma unroll
                    for (int ct = 0; ct < 2; ++ct)
                        pc[ct] = ((e[0] * acc[0][ct].x + e[1] * acc[0][ct].y) + (e[2] * acc[0][ct].z + e[3] * acc[0][ct].w)) +
                                 ((e[4] * acc[1][ct].x + e[5] * acc[1][ct].y) + (e[6] * acc[1][ct].z + e[7] * acc[1][ct].w));
                    s = red32(pc[0], pc[1]);
                    s = red16(s, s) * __builtin_amdgcn_rcpf(Ls);
                    if (!anynan(s)) break;
                    bool ok = vok(of[0][0]) && vok(of[0][1]) && vok(of[1][0]) && vok(of[1][1]);
#pragma unroll
                    for (int j = 0; j < 8; ++j) ok = ok && le[j] != INV;
                    if (__all(ok)) break;
                    if (layer == 0 && (it & 7) == 7) bump(true);
                    if (it > SPIN_CAP) break;
                }
                CFENCE();
                if (layer == 1) FST(39);
                if ((q & 1) == 0) *(LAS float*)(L + wa) = s;
            }
            BAR();
            if (layer == 1) FST(40);
            STAMP(9 + layer * 12);
            if (layer == 1 && b != 0) { FLUSH(); return; }
            {
                const int d0 = w * 128 + n * 8 + (q >> 1) * 4 + (q & 1) * 2;
                float x1a, x1b;
                const f2 xp = *(LAS f2*)(L + L_XS + d0 * 4);
                int o0 = WS_EA + layer * 16384 + q * 1024 + w * 256 + n * 16;
                PIN(o0);
                nap<D_EA>();
                for (int it = 0;; ++it) {
                    CFENCE();
                    v4u e[4];
#pragma unroll
                    for (int i = 0; i < 4; ++i) e[i] = ld16(o0 + i * 4096);
                    STAMP(10 + layer * 12);
                    float v[8];
#pragma unroll
                    for (int j = 0; j < 4; ++j) {
                        v[2 * j] = dsel(e[3][j], sel_lo, dsel(e[2][j], sel_lo, dsel(e[1][j], sel_lo, bflo(e[0][j]))));
                        v[2 * j + 1] = dsel(e[3][j], sel_hi, dsel(e[2][j], sel_hi, dsel(e[1][j], sel_hi, bfhi(e[0][j]))));
                    }
                    const float z0 = red16(red32(v[0], v[4]), red32(v[2], v[6])), z1 = red16(red32(v[1], v[5]), red32(v[3], v[7]));
                    const unsigned at = pk(z0, z1);
                    const unsigned x1p = pk(xp.x + bflo(at), xp.y + bfhi(at));
                    x1a = bflo(x1p); x1b = bfhi(x1p);
                    if (!anynan(x1a + x1b)) break;
                    if (__all(vok(e[0]) && vok(e[1]) && vok(e[2]) && vok(e[3]))) break;
                    if (layer == 0 && (it & 7) == 7) bump(true);
                    if (it > SPIN_CAP) break;
                }
                CFENCE();
                {
                    const float da = x1a - mu, db = x1b - mu;
                    float s = red32(da + db, da * da + db * db);
                    s = rowsum(red16(s, s));
                    if ((lane & 31) == 0) *(LAS float*)(L + L_ST1 + w * 8 + (lane >> 5) * 4) = s;
                }
                if (layer == 0) FST(42);
                BAR();
                if (layer == 0) FST(43);
                const f4 va = *(LAS f4*)(L + L_ST1), vb = *(LAS f4*)(L + L_ST1 + 16);
                const float S1 = ((va.x + va.z) + (vb.x + vb.z)) * (1.0f / 512.0f), S2 = (va.y + va.w) + (vb.y + vb.w);
                const float mu1 = mu + S1;
                const float var = mx2(fmaf(S2, 1.0f / 512.0f, -S1 * S1), 0.f);
                const float r1 = __builtin_amdgcn_rsqf(var + 1e-5f);
                const float rr = r1 * __builtin_amdgcn_rsqf(fmaf(var * r1, r1, 1e-5f));
                const unsigned hb = pk((x1a - mu1) * rr, (x1b - mu1) * rr);
                const unsigned ta = (hb & 0x7fffu) - (TE0 << 7), tb = ((hb >> 16) & 0x7fffu) - (TE0 << 7);
                const bool ina = ta < 2048u, inb = tb < 2048u;
                const unsigned ia = (ta & 2047u) | ((hb >> 4) & 2048u), ib = (tb & 2047u) | ((hb >> 20) & 2048u);
                float ya = bflo(*(LAS u16*)(L + L_TAB + ia * 2)), yb = bflo(*(LAS u16*)(L + L_TAB + ib * 2));
                if (__any(!(ina && inb))) {
#pragma nounroll
                    for (int half = 0; half < 2; ++half) {
                        unsigned long long miss = __ballot(half ? !inb : !ina);
                        while (miss) {
                            const int src = __builtin_ctzll(miss);
                            miss &= miss - 1;
                            const unsigned hs = (unsigned)__builtin_amdgcn_readlane((int)hb, src);
                            const float h = half ? bfhi(hs) : bflo(hs);
                            f2 ac2 = {0.f, 0.f};
#pragma nounroll
                            for (int f = lane; f < NF; f += 64)
                                ac2 = mixp(f2{h, h}, bflo(*(LAS u16*)(L + L_SO + f * 2)), bflo(*(LAS u16*)(L + L_SO + 4096 + f * 2)), bflo(*(LAS u16*)(L + L_SO + 8192 + f * 2)), ac2);
                            float acc = ac2.x;
                            acc = red32(acc, acc); acc = rowsum(red16(acc, acc));
                            const float yy = bfr(bfr(acc) + b2v);
                            if (lane == src) { if (half) yb = yy; else ya = yy; }
                        }
                    }
                }
                const unsigned x2p = pk(x1a + ya, x1b + yb);
                if (layer == 1) {
                    { const unsigned long long n_ = __builtin_amdgcn_s_memrealtime(); const unsigned long long u_ = n_ + 3 * (n_ - t_start); while (__builtin_amdgcn_s_memrealtime() < u_) __builtin_amdgcn_s_sleep(1); }
                    *(unsigned*)(a.out + d0) = x2p;
                    STAMP(11 + layer * 12); FLUSH();
                    return;
                }
                *(LAS unsigned*)(L + L_XB + d0 * 2) = x2p;
                *(LAS f2*)(L + L_XS + d0 * 4) = f2{bflo(x2p), bfhi(x2p)};
                FST(45);
                BAR();
                FST(46);
                STAMP(11 + layer * 12);
                mu = mu1;
            }
        }
    } else {
        const __amdgpu_buffer_rsrc_t rq = __builtin_amdgcn_make_buffer_rsrc((void*)(a.wq + (size_t)kb * 64 * 4096 + (g * 4 + hq) * 128), 0, 64 * 8192 - 7936, 0x00020000);
        const int ro_ = lane < 32 ? WS_R + g * 512 + lane * 16 : WS_R + 4096 + (lane & 7) * 128;
        v4u kst[8];
#pragma unroll
        for (int i = 0; i < 4; ++i) kst[i] = __builtin_amdgcn_raw_buffer_load_b128(rq, (w3 * 16 + i * 4 + q) * 8192 + n * 16, 0, 2);
        v4u xf[2];
#pragma unroll
        for (int ks = 0; ks < 2; ++ks) xf[ks] = *(const v4u*)(a.x + kb * 64 + ks * 32 + q * 8);
        const v4u x0v = *(const v4u*)(a.x + lane * 8);
        if (w < 7) {
            const __amdgpu_buffer_rsrc_t rp = __builtin_amdgcn_make_buffer_rsrc((void*)(w == 4 ? a.w1 : w == 5 ? a.b1 : a.w2), 0, NF * 2, 0x00020000);
#pragma unroll
            for (int i = 0; i < 4; ++i) kst[4 + i] = __builtin_amdgcn_raw_buffer_load_b128(rp, i * 1024 + lane * 16, 0, 0);
        } else {
            const v4u iv = {INV, INV, INV, INV};
            st16(WS_EB + (lane >> 5) * 131072 + g * 16384 + kb * 2048 + hq * 512 + (lane & 31) * 16, iv);
            int o;
            if (lane < 32) o = WS_EB + 2 * 131072 + g * 16384 + kb * 2048 + hq * 512 + lane * 16;
            else if (lane < 40) o = WS_EA + ((lane >> 2) & 1) * 16384 + (g * 2 + hp) * 1024 + cb * 64 + (lane & 3) * 16;
            else if (lane < 48) o = WS_EL + ((lane >> 2) & 1) * 65536 + g * 8192 + (lane & 3) * 2048 + r * 64;
            else o = WS_TAB + b * 32 + (lane & 1) * 16;
            if (lane < 50) st16(o, iv);
            st16(WS_EC + g * 32768 + (lane >> 2) * 2048 + r * 64 + (lane & 3) * 16, iv);
            st16(WS_EC + 262144 + g * 32768 + (lane >> 2) * 2048 + r * 64 + (lane & 3) * 16, iv);
        }
        const float b2v = bflo(a.b2[0]);
        BAR();
#pragma unroll
        for (int i = 0; i < 4; ++i) { const int row = w3 * 16 + i * 4 + q;
            *(LAS v4u*)(L + L_SQ + row * 256 + ((n ^ (((row & 3) << 2) | ((row >> 2) & 3))) * 16)) = kst[i]; }
        if (w < 7) {
#pragma unroll
            for (int i = 0; i < 4; ++i) *(LAS v4u*)(L + L_SO + w3 * 4096 + i * 1024 + lane * 16) = kst[4 + i];
        } else {
            asm volatile("s_waitcnt vmcnt(0)" ::: "memory");
            const v2u t = __builtin_bit_cast(v2u, (unsigned long long)__builtin_amdgcn_s_memrealtime());
            if (lane == 0) st16(WS_R + g * 512 + r * 16, v4u{t.x, t.y, t.x, t.y});
        }
        if (n == 1) { xf[0] = v4u{0x3f803f80u, 0x3f803f80u, 0x3f803f80u, 0x3f803f80u}; xf[1] = xf[0]; }
        asm volatile("" :: "v"(cpf), "v"(cpf2));
        STAMP(1);
        asm volatile("s_waitcnt lgkmcnt(0)" ::: "memory");
        if (lane == 0) __hip_atomic_fetch_add((LAS unsigned*)(L + L_GATE), 1u, __ATOMIC_RELAXED, __HIP_MEMORY_SCOPE_WORKGROUP);
        while (*gate < 4u) __builtin_amdgcn_s_sleep(1);
        CFENCE();
        v4u wqf[2][2];
        {
            const int aa = n >> 2, p = n & 3;
#pragma unroll
            for (int ct = 0; ct < 2; ++ct)
#pragma unroll
                for (int ks = 0; ks < 2; ++ks) {
                    v2u t[2];
#pragma unroll
                    for (int tt = 0; tt < 2; ++tt) { const int row = ks * 32 + q * 8 + tt * 4 + aa, ch = 4 * w3 + 2 * ct + (p >> 1);
                        t[tt] = trd(L + L_SQ + row * 256 + ((ch ^ (((row & 3) << 2) | ((row >> 2) & 3))) * 16) + 8 * (p & 1)); }
                    wqf[ct][ks] = v4u{t[0].x, t[0].y, t[1].x, t[1].y};
                }
#pragma unroll
            for (int ct = 0; ct < 2; ++ct) {
                const f4 c = mfma32(wqf[ct][1], xf[1], mfma32(wqf[ct][0], xf[0], f4{0, 0, 0, 0}));
                if (n < 2) st16(WS_EB + n * 2 * 131072 + g * 16384 + kb * 2048 + hq * 512 + w3 * 128 + ct * 64 + q * 16, __builtin_bit_cast(v4u, c));
            }
        }
        SB();
        STAMP(2);
        f2 csum = {0.f, 0.f};
        auto fetch_q = [&](int layer, auto&& meanwhile) {
            int o0 = WS_EB + layer * 131072 + g * 16384 + (lane >> 5) * 2048 + w3 * 512 + (lane & 31) * 16;
            int qa = L_QS + (w3 * 128 + (lane & 31) * 4 + (lane >> 5) * 2) * 2;
            PIN(o0); PIN(qa);
            f2 qv;
            if (layer == 1) nap<D_EB>();
            bool ready = layer == 1;
            for (int it = 0;; ++it) {
                CFENCE();
                v4u e[4], c[4], rec = {0, 0, 0, 0};
                unsigned long long lim = 0;
                if (layer == 0) { lim = __builtin_amdgcn_s_memrealtime() - MARGIN; SB(); if (!ready && lane < 32) rec = ld16(ro_); }
#pragma unroll
                for (int i = 0; i < 4; ++i) e[i] = ld16(o0 + i * 4096);
                if (layer == 0) {
#pragma unroll
                    for (int i = 0; i < 4; ++i) c[i] = ld16(o0 + 2 * 131072 + i * 4096);
                    SB();
                    if (it == 0) meanwhile();
                    SB();
                    const f4 cs = (__builtin_bit_cast(f4, c[0]) + __builtin_bit_cast(f4, c[1])) + (__builtin_bit_cast(f4, c[2]) + __builtin_bit_cast(f4, c[3]));
                    csum = f2{red32(cs.x, cs.z), red32(cs.y, cs.w)};
                    if (!ready) ready = __all(lane >= 32 || within(rec.x, rec.y, lim) || within(rec.z, rec.w, lim));
                }
                const f4 es = (__builtin_bit_cast(f4, e[0]) + __builtin_bit_cast(f4, e[1])) + (__builtin_bit_cast(f4, e[2]) + __builtin_bit_cast(f4, e[3]));
                qv = (f2{red32(es.x, es.z), red32(es.y, es.w)} - csum * mu) * rsig;
                if (ready) {
                    if (!anynan(qv.x + qv.y)) break;
                    bool ok = vok(e[0]) && vok(e[1]) && vok(e[2]) && vok(e[3]);
                    if (layer == 0) ok = ok && vok(c[0]) && vok(c[1]) && vok(c[2]) && vok(c[3]);
                    if (__all(ok)) break;
                }
                if (layer == 0 && (it & 7) == 7) bump(false);
                if (it > SPIN_CAP) break;
            }
            CFENCE();
            *(LAS unsigned*)(L + qa) = pk(qv.x, qv.y);
        };
        float w1f[7], b1f[7], w2f[7];
            f2 tacc[8];
            const unsigned hbits0 = (unsigned)((b >> 7) << 15) | (unsigned)((TE0 << 7) + (b & 127) * 16);
            auto tab_pair = [&](int e) {
                const f2 h = {u2f((hbits0 + e) << 16), u2f((hbits0 + e + 8) << 16)};
                f2 acc = {0.f, 0.f};
#pragma unroll
                for (int j = 0; j < 7; ++j) acc = mixp(h, w1f[j], b1f[j], w2f[j], acc);
                asm volatile("" : "+v"(acc));
                tacc[e] = acc;
            };
#pragma unroll
            for (int j = 0; j < 7; ++j) { const int f = ((tid & 255) + 256 * j) * 2;
                w1f[j] = bflo(*(LAS u16*)(L + L_SO + f)); b1f[j] = bflo(*(LAS u16*)(L + L_SO + 4096 + f)); w2f[j] = bflo(*(LAS u16*)(L + L_SO + 8192 + f)); }
            ln_stats(f4{bflo(x0v.x), bfhi(x0v.x), bflo(x0v.y), bfhi(x0v.y)}, f4{bflo(x0v.z), bfhi(x0v.z), bflo(x0v.w), bfhi(x0v.w)});
            SB();
#pragma unroll
            for (int e = 0; e < TG; ++e) tab_pair(e);
            SB();
            STAMP(3);
            fetch_q(0, [&]() {
#pragma unroll
                for (int e = TG; e < TA; ++e) tab_pair(e);
            });
            if (w == 7) bump(true);
            STAMP(4);
            BAR();
#pragma unroll
            for (int e = TA; e < TB2; ++e) tab_pair(e);
            STAMP(5);
#pragma unroll
        for (int layer = 0; layer < 2; ++layer) {
            if (layer == 1) BAR();
            int ea = WS_EC + layer * 262144 + g * 32768 + (w3 >> 1) * 16384 + ((w3 & 1) * 4 + q) * 2048 + r * 64 + (n >> 2) * 16;
            int oa = L_OW + w3 * 528 + lane * 8;
            PIN(ea); PIN(oa);
            BAR();
            if (layer == 1) FST(36);
            {
                const f4 ma = *(LAS f4*)(L + L_M + w3 * 32), la = *(LAS f4*)(L + L_M + w3 * 32 + 16);
                f2 v[4];
#pragma unroll
                for (int ww = 0; ww < 4; ++ww) v[ww] = *(LAS f2*)(L + oa + ww * 2112);
                const float M = mx2(mx3(ma.x, ma.y, ma.z), ma.w);
                const float e0 = __builtin_amdgcn_exp2f(ma.x - M), e1 = __builtin_amdgcn_exp2f(ma.y - M), e2 = __builtin_amdgcn_exp2f(ma.z - M), e3 = __builtin_amdgcn_exp2f(ma.w - M);
                const float Ls = (e0 * la.x + e1 * la.y) + (e2 * la.z + e3 * la.w);
                const f2 oo = (v[0] * e0 + v[1] * e1) + (v[2] * e2 + v[3] * e3);
                const float inv = __builtin_amdgcn_rcpf(Ls);
                const unsigned wd = nn(pk(oo.x * inv, oo.y * inv));
                const v4u vv = {wd, dppu<0x55>(wd), dppu<0xAA>(wd), dppu<0xFF>(wd)};
                if ((lane & 3) == 0) st16(ea, vv);
                if (lane == 0) st4(WS_EL + layer * 65536 + g * 8192 + w3 * 2048 + r * 64, f2u(M + __builtin_amdgcn_logf(Ls)));
            }
            STAMP(7 + layer * 12);
            if (layer == 0) {
#pragma unroll
                for (int e = TB2; e < 8; ++e) tab_pair(e);
                float t8[8], t4[4];
#pragma unroll
                for (int j = 0; j < 8; ++j) t8[j] = red32(tacc[j].x, tacc[j].y);
#pragma unroll
                for (int j = 0; j < 4; ++j) t4[j] = rowsum(red16(t8[j], t8[j + 4]));
                if (n == 0) *(LAS f4*)(L + L_TR + w3 * 64 + q * 16) = f4{t4[0], t4[1], t4[2], t4[3]};
                STAMP(6);
                if (w == 7) {
                    for (int it = 0;; ++it) {
                        CFENCE();
                        v4u cur = {0, 0, 0, 0};
                        if (lane >= 32) cur = ld16(ro_);
                        if (__all(lane < 32 || within(cur.x, cur.y, __builtin_amdgcn_s_memrealtime()))) break;
                        if ((it & 3) == 3) bump(true);
                        if (it > SPIN_CAP) break;
                        nap<16>();
                    }
                    CFENCE();
                }
            }
            BAR();
            if (w == 4 && lane < 16) {
                f2 v[4];
#pragma unroll
                for (int ww = 0; ww < 4; ++ww) v[ww] = *(LAS f2*)(L + L_WR + ww * 128 + lane * 8);
                const f2 sv = (v[0] + v[1]) + (v[2] + v[3]);
                const unsigned wd = nn(pk(sv.x, sv.y));
                const v4u vv = {wd, dppu<0x55>(wd), dppu<0xAA>(wd), dppu<0xFF>(wd)};
                if ((lane & 3) == 0) st16(WS_EA + layer * 16384 + (g * 2 + hp) * 1024 + cb * 64 + lane * 4, vv);
            }
            if (layer == 0 && w == 5 && lane < 8) {
                f2 tv[4];
#pragma unroll
                for (int ww = 0; ww < 4; ++ww) tv[ww] = *(LAS f2*)(L + L_TR + ww * 64 + lane * 8);
                const f2 sv = (tv[0] + tv[1]) + (tv[2] + tv[3]);
                const unsigned wd = nn(pk(bfr(sv.x) + b2v, bfr(sv.y) + b2v));
                const v4u vv = {wd, dppu<0x55>(wd), dppu<0xAA>(wd), dppu<0xFF>(wd)};
                if ((lane & 3) == 0) st16(WS_TAB + b * 32 + lane * 4, vv);
            }
            STAMP(9 + layer * 12);
            if (layer == 1) { FLUSH(); return; }
            nap<D_EA>();
            for (int it = 0;; ++it) {
                CFENCE();
                const v4u t0 = ld16(WS_TAB + w3 * 2048 + lane * 16), t1 = ld16(WS_TAB + w3 * 2048 + 1024 + lane * 16);
                *(LAS v4u*)(L + L_TAB + w3 * 2048 + lane * 16) = t0; *(LAS v4u*)(L + L_TAB + w3 * 2048 + 1024 + lane * 16) = t1;
                if (__all(vok(t0) && vok(t1))) break;
                if ((it & 7) == 7) bump(true);
                if (it > SPIN_CAP) break;
            }
            CFENCE();
            int qo = WS_EB + 131072 + g * 16384 + kb * 2048 + hq * 512 + w3 * 128 + n * 64 + q * 16;
            int xa_ = L_XB + kb * 128 + q * 16;
            PIN(qo); PIN(xa_);
            BAR();
            BAR();
            {
                const v4u xa = *(LAS v4u*)(L + xa_), xb = *(LAS v4u*)(L + xa_ + 64);
                const f4 c0 = mfma32(wqf[0][1], xb, mfma32(wqf[0][0], xa, f4{0, 0, 0, 0})), c1 = mfma32(wqf[1][1], xb, mfma32(wqf[1][0], xa, f4{0, 0, 0, 0}));
                if (n < 2) st16(qo, __builtin_bit_cast(v4u, n == 0 ? c0 : c1));
            }
            STAMP(11 + layer * 12);
            SB();
            ln_stats(*(LAS f4*)(L + L_XS + lane * 32), *(LAS f4*)(L + L_XS + lane * 32 + 16));
            fetch_q(1, []() {});
            STAMP(16);
        }
    }
}

}

extern "C" void kernel_launch(void* const* d_in, const int* in_sizes, int n_in,
                              void* d_out, int out_size, void* d_ws, size_t ws_size,
                              hipStream_t stream) {
    fz::Args a;
    a.x = (const u16*)d_in[0]; a.kc = (const u16*)d_in[1]; a.vc = (const u16*)d_in[2];
    a.wq = (const u16*)d_in[3]; a.wo = (const u16*)d_in[4];
    a.w1 = (const u16*)d_in[6]; a.b1 = (const u16*)d_in[7]; a.w2 = (const u16*)d_in[8]; a.b2 = (const u16*)d_in[9];
    a.out = (u16*)d_out; a.ws = (unsigned char*)d_ws;
    (void)hipFuncSetAttribute((const void*)fz::fused, hipFuncAttributeMaxDynamicSharedMemorySize, fz::LDS_BYTES);
    fz::fused<<<256, 512, fz::LDS_BYTES, stream>>>(a);
}
```

```cpp
#include <hip/hip_runtime.h>
#include <hip/hip_bf16.h>
#include <stdint.h>

typedef unsigned short u16;
typedef unsigned v4u __attribute__((ext_vector_type(4)));
typedef unsigned v2u __attribute__((ext_vector_type(2)));
typedef float f4 __attribute__((ext_vector_type(4)));
typedef float f2 __attribute__((ext_vector_type(2)));
typedef short s4 __attribute__((ext_vector_type(4)));
typedef __bf16 b8 __attribute__((ext_vector_type(8)));
typedef __bf16 b2 __attribute__((ext_vector_type(2)));
#define LAS __attribute__((address_space(3)))

namespace fz {

constexpr int SK = 65536, WIN = 4096, NF = 1792;
constexpr unsigned INV = 0xFFFFFFFFu;
constexpr int WS_R = 0;
constexpr int WS_TAB = 1 << 20;
constexpr int WS_EB = 16384;
constexpr int WS_EC = WS_EB + 3 * 65536;
constexpr int WS_EL = WS_EC + 2 * 262144;
constexpr int WS_EA = WS_EL + 2 * 65536;
constexpr int WS_EH = WS_TAB + 16384;
constexpr int WS_END = WS_EH + 16384 + 1024;
static_assert(WS_EA + 2 * 16384 <= WS_TAB, "ws");
constexpr int L_STG = 0;
constexpr int L_SQ = 32768;
constexpr int L_SO = L_SQ + 16384;
constexpr int L_TAB = L_SO + 16384;
constexpr int L_OW = L_TAB + 12288;
constexpr int L_QS = L_OW + 4 * 4 * 132 * 4;
constexpr int L_XB = L_QS + 1024;
constexpr int L_XS = L_XB + 1024;
constexpr int L_M = L_XS + 2048;
constexpr int L_WR = L_M + 128;
constexpr int L_ST1 = L_WR + 512;
constexpr int L_ST2 = L_ST1 + 64;
constexpr int L_TR = L_ST2 + 64;
constexpr int L_GATE = L_TR + 512;
constexpr int L_WO = L_GATE + 48;
constexpr int L_END = L_WO + 16384;
constexpr int LDS_BYTES = 112 * 1024;
static_assert(L_END <= LDS_BYTES, "lds");
constexpr int TE0 = 107;
constexpr int SPIN_CAP = 1 << 22;
constexpr unsigned long long MARGIN = 20;
constexpr int WN = 4, VS = 3, D_K = 16, D_G = 34, D_EB = 2, D_EC = 14, D_EA1 = 10, H_NAP = 3;
constexpr unsigned H_EC = 120, H_TAB = 120, H_EA = 260, C_EB = 415, C_EC = 555, C_EA = 680;
constexpr int CODE_PF = 23552;

__device__ __forceinline__ float u2f(unsigned u) { return __builtin_bit_cast(float, u); }
__device__ __forceinline__ unsigned f2u(float f) { return __builtin_bit_cast(unsigned, f); }
__device__ __forceinline__ float bflo(unsigned u) { return u2f(u << 16); }
__device__ __forceinline__ float bfhi(unsigned u) { return u2f(u & 0xffff0000u); }
__device__ __forceinline__ float bfr(float x) { return (float)(__bf16)x; }
__device__ __forceinline__ unsigned pk(float lo, float hi) { b2 v = {(__bf16)lo, (__bf16)hi}; return __builtin_bit_cast(unsigned, v); }
__device__ __forceinline__ unsigned nn(unsigned w) { return w == INV ? 0x7fc07fc0u : w; }
__device__ __forceinline__ f4 mfma32(v4u a, v4u b, f4 c) { return __builtin_amdgcn_mfma_f32_16x16x32_bf16(__builtin_bit_cast(b8, a), __builtin_bit_cast(b8, b), c, 0, 0, 0); }
__device__ __forceinline__ v2u trd(LAS unsigned char* p) { return __builtin_bit_cast(v2u, __builtin_amdgcn_ds_read_tr16_b64_v4i16((LAS s4*)p)); }
template <int CTRL> __device__ __forceinline__ float dpp(float x) { return u2f(__builtin_amdgcn_mov_dpp(f2u(x), CTRL, 0xf, 0xf, true)); }
template <int CTRL> __device__ __forceinline__ unsigned dppu(unsigned x) { return (unsigned)__builtin_amdgcn_mov_dpp((int)x, CTRL, 0xf, 0xf, true); }
constexpr int ROR8 = 0x128, ROR4 = 0x124, ROR2 = 0x122, ROR1 = 0x121;
__device__ __forceinline__ float red32(float a, float b) { auto r = __builtin_amdgcn_permlane32_swap(f2u(a), f2u(b), false, false); return u2f(r[0]) + u2f(r[1]); }
__device__ __forceinline__ float red16(float a, float b) { auto r = __builtin_amdgcn_permlane16_swap(f2u(a), f2u(b), false, false); return u2f(r[0]) + u2f(r[1]); }
__device__ __forceinline__ float mx2(float a, float b) { return fmaxf(a, b); }
__device__ __forceinline__ float mx3(float a, float b, float c) { return fmaxf(fmaxf(a, b), c); }
__device__ __forceinline__ float max32(float a) { auto r = __builtin_amdgcn_permlane32_swap(f2u(a), f2u(a), false, false); return mx2(u2f(r[0]), u2f(r[1])); }
__device__ __forceinline__ float max16(float a) { auto r = __builtin_amdgcn_permlane16_swap(f2u(a), f2u(a), false, false); return mx2(u2f(r[0]), u2f(r[1])); }
__device__ __forceinline__ float dsel(unsigned p, unsigned sel, float acc) { return __builtin_amdgcn_fdot2_f32_bf16(__builtin_bit_cast(b2, p), __builtin_bit_cast(b2, sel), acc, false); }
__device__ __forceinline__ f2 mixp(f2 h, float w1, float b1, float w2, f2 acc) {
    const float c0 = -2.0f * 0.7978845608028654f * 1.4426950408889634f, c1 = c0 * 0.044715f;
    const f2 u = h * w1 + b1;
    const f2 z = u * (u * u * c1 + c0);
    const f2 d = f2{__builtin_amdgcn_exp2f(z.x), __builtin_amdgcn_exp2f(z.y)} + 1.0f;
    const f2 gq = u * f2{__builtin_amdgcn_rcpf(d.x), __builtin_amdgcn_rcpf(d.y)};
    return gq * w2 + acc;
}
__device__ __forceinline__ bool anynan(float x) { return __any(x != x); }
#define PIN(x) asm volatile("" : "+v"(x))
__device__ __forceinline__ float rowsum(float x) { x += dpp<ROR8>(x); x += dpp<ROR4>(x); x += dpp<ROR2>(x); x += dpp<ROR1>(x); return x; }
#define BAR() do { __builtin_amdgcn_sched_barrier(0); asm volatile("s_waitcnt lgkmcnt(0)\n\ts_barrier" ::: "memory"); __builtin_amdgcn_sched_barrier(0); } while (0)
#define CFENCE() asm volatile("" ::: "memory")
#define SB() __builtin_amdgcn_sched_barrier(0)
template <int N> __device__ __forceinline__ void nap() {
#pragma unroll
    for (int i = 0; i < N / 8; ++i) __builtin_amdgcn_s_sleep(8);
    if (N % 8) __builtin_amdgcn_s_sleep(N % 8);
}
__device__ __forceinline__ bool vok(v4u a) { return a.x != INV && a.y != INV && a.z != INV && a.w != INV; }

struct Args {
    const u16 *x, *kc, *vc, *wq, *wo, *w1, *b1, *w2, *b2;
    u16* out;
    unsigned char* ws;
};

__global__ void __launch_bounds__(768, 3) fused(Args a) {
    extern __shared__ __attribute__((aligned(16))) unsigned char smem[];
    LAS unsigned char* L = (LAS unsigned char*)smem;
    const int b = blockIdx.x, tid = threadIdx.x;
    unsigned cpf;
    {
        unsigned long long pc;
        unsigned co;
        asm volatile("s_getpc_b64 %2\n\tv_lshlrev_b32 %1, 6, %3\n\tv_min_u32 %1, %4, %1\n\tglobal_load_dword %0, %1, %2"
                     : "=&v"(cpf), "=&v"(co), "=&s"(pc) : "v"(tid), "i"(CODE_PF - 64) : "memory");
        SB();
    }
    const int w = __builtin_amdgcn_readfirstlane(tid >> 6), lane = tid & 63;
    const int w3 = w & 3;
    const int g = b & 7, r = b >> 3;
    const int kb = r & 3, cq = r >> 2, hp = r >> 4, cb = r & 15;
    const int n = lane & 15, q = lane >> 4;
    const __amdgpu_buffer_rsrc_t rs = __builtin_amdgcn_make_buffer_rsrc((void*)a.ws, 0, WS_END, 0x00020000);
    auto ld16 = [&](int off) -> v4u { return __builtin_amdgcn_raw_buffer_load_b128(rs, off, 0, 16); };
    auto ld4 = [&](int off) -> unsigned { return __builtin_amdgcn_raw_buffer_load_b32(rs, off, 0, 16); };
    auto st16 = [&](int off, v4u v) { __builtin_amdgcn_raw_buffer_store_b128(v, rs, off, 0, 16); };
    auto st4 = [&](int off, unsigned v) { __builtin_amdgcn_raw_buffer_store_b32(v, rs, off, 0, 16); };
    const unsigned long long t_start = __builtin_amdgcn_s_memrealtime();
    auto bump = [&](bool tok) {
        if (*(volatile LAS unsigned*)(L + L_GATE + 32) < 4u) return;
        const unsigned long long t = __builtin_amdgcn_s_memrealtime();
        if (lane == 0 || (lane == 1 && r == 0 && tok))
            __builtin_amdgcn_raw_buffer_store_b64(__builtin_bit_cast(v2u, t), rs, lane == 0 ? WS_R + g * 512 + r * 16 + 8 : WS_R + 4096 + g * 128, 0, 16);
    };
    auto within = [&](unsigned lo, unsigned hi, unsigned long long lim) -> bool { return (((unsigned long long)hi << 32) | lo) - t_start <= lim - t_start; };
    auto gather_hints = [&](int addr, int slot) {
        const unsigned base = (unsigned)t_start;
        unsigned tm;
        for (int it = 0;; ++it) {
            CFENCE();
            tm = ld4(addr) - base;
            if (__all(tm <= (unsigned)__builtin_amdgcn_s_memrealtime() - base)) break;
            if ((it & 15) == 15) bump(true);
            if (it > 2048) { tm = 0; break; }
            nap<H_NAP>();
        }
        float f = (float)tm;
        f = max32(max16(f)); f = fmaxf(f, dpp<ROR8>(f)); f = fmaxf(f, dpp<ROR4>(f)); f = fmaxf(f, dpp<ROR2>(f)); f = fmaxf(f, dpp<ROR1>(f));
        if (lane == 0) *(volatile LAS unsigned*)(L + L_GATE + slot * 4) = base + (unsigned)f + 1u;
        if (lane == 0) *(volatile LAS unsigned*)(L + L_GATE + slot * 4 + 8) = 1u;
    };
    auto until = [&](int slot, unsigned off) {
        while (*(volatile LAS unsigned*)(L + L_GATE + slot * 4 + 8) == 0u) __builtin_amdgcn_s_sleep(1);
        const unsigned tgt = __builtin_amdgcn_readfirstlane(*(volatile LAS unsigned*)(L + L_GATE + slot * 4)) + off;
        while ((int)((unsigned)__builtin_amdgcn_s_memrealtime() - tgt) < 0) __builtin_amdgcn_s_sleep(1);
    };
    volatile LAS unsigned* gate = (volatile LAS unsigned*)(L + L_GATE);
    if (w == 0) { *(LAS v4u*)(L + L_GATE) = v4u{0u, 0u, 0u, 0u}; *(LAS v4u*)(L + L_GATE + 16) = v4u{0u, 0u, 0u, 0u}; *(LAS v4u*)(L + L_GATE + 32) = v4u{0u, 0u, 0u, 0u}; }

    float mu, rsig;
    auto ln_stats = [&](f4 xa, f4 xb) {
        float s = ((xa.x + xa.y) + (xa.z + xa.w)) + ((xb.x + xb.y) + (xb.z + xb.w));
        s = red32(s, s); s = red16(s, s); s = rowsum(s);
        mu = s * (1.0f / 512.0f);
        const f4 da = xa - mu, db = xb - mu;
        float v = ((da.x * da.x + da.y * da.y) + (da.z * da.z + da.w * da.w)) + ((db.x * db.x + db.y * db.y) + (db.z * db.z + db.w * db.w));
        v = red32(v, v); v = red16(v, v); v = rowsum(v);
        rsig = __builtin_amdgcn_rcpf(bfr(__builtin_amdgcn_sqrtf(bfr(bfr(v * (1.0f / 512.0f)) + 1e-5f))));
        mu = bfr(mu);
    };

    if (w < 4) {
        __builtin_amdgcn_s_setprio(3);
        const size_t kvb = ((size_t)g * SK + (SK - WIN) + r * 128 + w * 4) * 128;
        const __amdgpu_buffer_rsrc_t rk = __builtin_amdgcn_make_buffer_rsrc((void*)(a.kc + kvb), 0, 32768 - 3072, 0x00020000);
        const __amdgpu_buffer_rsrc_t rv = __builtin_amdgcn_make_buffer_rsrc((void*)(a.vc + kvb), 0, 32768 - 3072, 0x00020000);
        unsigned sel_lo = 0x00003f80u, sel_hi = 0x3f800000u;
        asm volatile("" : "+s"(sel_lo), "+s"(sel_hi));
        const unsigned mk0 = (n >> 2) == 0 ? ~0u : 0u, mk1 = (n >> 2) == 1 ? ~0u : 0u, mk2 = (n >> 2) == 2 ? ~0u : 0u, mk3 = (n >> 2) == 3 ? ~0u : 0u;
        LAS unsigned char* stg = L + L_STG + w * 8192;
        const v4u x0v = *(const v4u*)(a.x + lane * 8);
        BAR();
        nap<D_K>();
        v4u sb[16];
        auto issue = [&](int j) {
            if (j < 8) sb[j] = __builtin_amdgcn_raw_buffer_load_b128(rk, j * 4096 + lane * 16, 0, 2);
            else if (j < 16) sb[j] = __builtin_amdgcn_raw_buffer_load_b128(rv, (j - 8) * 4096 + lane * 16, 0, 2);
            SB();
        };
#pragma unroll
        for (int j = 0; j < WN; ++j) issue(j);
        {
            const f4 xa = {bflo(x0v.x), bfhi(x0v.x), bflo(x0v.y), bfhi(x0v.y)}, xb = {bflo(x0v.z), bfhi(x0v.z), bflo(x0v.w), bfhi(x0v.w)};
            ln_stats(xa, xb);
            if (w == 2) { *(LAS f4*)(L + L_XS + lane * 32) = xa; *(LAS f4*)(L + L_XS + lane * 32 + 16) = xb; }
        }
#pragma unroll
        for (int i = 0; i < 8; ++i) {
            const int row = i * 4 + q; *(LAS v4u*)(stg + row * 256 + ((n ^ (row & 15)) * 16)) = sb[i];
            SB();
            issue(i + WN);
        }
        asm volatile("" :: "v"(cpf) : "memory");
        v4u kf[2][4], vf[8], wof[2][2];
#pragma unroll
        for (int t = 0; t < 2; ++t)
#pragma unroll
            for (int ks = 0; ks < 4; ++ks) kf[t][ks] = *(LAS v4u*)(stg + (t * 16 + n) * 256 + (((ks * 4 + q) ^ n) * 16));
        SB();
#pragma unroll
        for (int i = 0; i < 8; ++i) {
            const int row = i * 4 + q; *(LAS v4u*)(stg + row * 256 + ((n ^ ((row & 7) << 1)) * 16)) = sb[8 + i];
            SB();
            issue(8 + i + WN);
            if (i == VS) gate[10] = 1u;
        }
        {
            const int row = q * 4 + (n >> 2), p = n & 3;
#pragma unroll
            for (int c = 0; c < 8; ++c) {
                LAS unsigned char* ap = stg + row * 256 + (((2 * c + (p >> 1)) ^ ((row & 7) << 1)) * 16) + 8 * (p & 1);
                const v2u t0 = trd(ap), t1 = trd(ap + 4096);
                vf[c] = v4u{t0.x, t0.y, t1.x, t1.y};
            }
        }

#pragma nounroll
        for (int lyr = 0; lyr < 2; ++lyr) {
            int layer = __builtin_amdgcn_readfirstlane(lyr);
            asm volatile("" : "+s"(layer));
            int lane_o = tid & 63;
            asm volatile("" : "+v"(lane_o));
            const int lane = lane_o, n = lane & 15, q = lane >> 4;
            if (layer == 0) {
                while (gate[5] < 4u) __builtin_amdgcn_s_sleep(1);
                CFENCE();
                if (w == 0 && lane == 0) st4(WS_EH + g * 2048 + r * 64, (unsigned)__builtin_amdgcn_s_memrealtime());
            } else BAR();
            {
                v4u qf[4];
#pragma unroll
                for (int ks = 0; ks < 4; ++ks) qf[ks] = *(LAS v4u*)(L + L_QS + (n & 3) * 256 + ks * 64 + q * 16);
                SB();
                f4 s0 = {0, 0, 0, 0}, s1 = {0, 0, 0, 0};
#pragma unroll
                for (int ks = 0; ks < 4; ++ks) { s0 = mfma32(kf[0][ks], qf[ks], s0); s1 = mfma32(kf[1][ks], qf[ks], s1); }
                const float C2 = 0.08838834764831845f * 1.4426950408889634f;
                s0 *= C2; s1 *= C2;
                float m = fmaxf(fmaxf(fmaxf(s0.x, s0.y), fmaxf(s0.z, s0.w)), fmaxf(fmaxf(s1.x, s1.y), fmaxf(s1.z, s1.w)));
                m = max32(max16(m));
                float e[8];
                e[0] = __builtin_amdgcn_exp2f(s0.x - m); e[1] = __builtin_amdgcn_exp2f(s0.y - m);
                e[2] = __builtin_amdgcn_exp2f(s0.z - m); e[3] = __builtin_amdgcn_exp2f(s0.w - m);
                e[4] = __builtin_amdgcn_exp2f(s1.x - m); e[5] = __builtin_amdgcn_exp2f(s1.y - m);
                e[6] = __builtin_amdgcn_exp2f(s1.z - m); e[7] = __builtin_amdgcn_exp2f(s1.w - m);
                const v4u pf = {pk(e[0], e[1]), pk(e[2], e[3]), pk(e[4], e[5]), pk(e[6], e[7])};
                f4 oa = {0, 0, 0, 0}, ob = {0, 0, 0, 0};
                { const v4u pm = pf & mk0; oa = mfma32(vf[0], pm, oa); ob = mfma32(vf[4], pm, ob); }
                { const v4u pm = pf & mk1; oa = mfma32(vf[1], pm, oa); ob = mfma32(vf[5], pm, ob); }
                { const v4u pm = pf & mk2; oa = mfma32(vf[2], pm, oa); ob = mfma32(vf[6], pm, ob); }
                { const v4u pm = pf & mk3; oa = mfma32(vf[3], pm, oa); ob = mfma32(vf[7], pm, ob); }
                float ls = ((e[0] + e[1]) + (e[2] + e[3])) + ((e[4] + e[5]) + (e[6] + e[7]));
                ls = red32(ls, ls); ls = red16(ls, ls);
                if (lane < 4) { *(LAS float*)(L + L_M + n * 32 + w * 4) = m; *(LAS float*)(L + L_M + n * 32 + 16 + w * 4) = ls; }
                *(LAS f4*)(L + L_OW + (w * 4 + (n & 3)) * 528 + (n >> 2) * 64 + q * 16) = oa;
                *(LAS f4*)(L + L_OW + (w * 4 + (n & 3)) * 528 + (n >> 2) * 64 + 256 + q * 16) = ob;
            }
            if (layer == 0) {
                asm volatile("s_waitcnt lgkmcnt(0)" ::: "memory");
                if (lane == 0) __hip_atomic_fetch_add((LAS unsigned*)(L + L_GATE + 36), 1u, __ATOMIC_RELAXED, __HIP_MEMORY_SCOPE_WORKGROUP);
            } else BAR();
            {
                int o0 = WS_EC + layer * 262144 + g * 32768 + hp * 16384 + w * 4096 + n * 64 + q * 16;
                int l0 = WS_EL + layer * 65536 + g * 8192 + (hp * 2 + (w >> 1)) * 2048 + q * 256;
                int wa = L_WR + w * 128 + (q >> 1) * 64 + n * 4;
                PIN(o0); PIN(l0); PIN(wa);
                if (layer == 0) {
                    while (gate[11] < 4u) __builtin_amdgcn_s_sleep(1);
                    CFENCE();
                    const int aa = n >> 2, p = n & 3;
#pragma unroll
                    for (int ks = 0; ks < 2; ++ks)
#pragma unroll
                        for (int ct = 0; ct < 2; ++ct) {
                            v2u t[2];
#pragma unroll
                            for (int tt = 0; tt < 2; ++tt) { const int row = ks * 32 + q * 8 + tt * 4 + aa;
                                t[tt] = trd(L + L_WO + w * 4096 + row * 64 + (((2 * ct + (p >> 1)) ^ (((row >> 3) & 1) << 1)) * 16) + 8 * (p & 1)); }
                            wof[ks][ct] = v4u{t[0].x, t[0].y, t[1].x, t[1].y};
                        }
                    if (w == 0) {
                        gather_hints(WS_EH + g * 2048 + (lane & 31) * 64, 1);
                        if (r == 0 && lane == 0) st4(WS_EH + 16384 + g * 128, *(volatile LAS unsigned*)(L + L_GATE + 4));
                    }
                    until(1, H_EC);
                } else { nap<D_EC>(); until(2, C_EC); }
                float s;
                for (int it = 0;; ++it) {
                    if (it > 0) {
                        for (int i2 = 0;; ++i2) {
                            CFENCE();
                            const unsigned f0 = ld4(l0 - q * 256 + (lane & 31) * 64);
                            if (__all(f0 != INV)) break;
                            if (layer == 0 && (i2 & 7) == 7) bump(true);
                            if (i2 > SPIN_CAP) break;
                            nap<4>();
                        }
                    }
                    CFENCE();
                    v4u of[2][2];
                    unsigned le[8];
#pragma unroll
                    for (int j = 0; j < 8; ++j) le[j] = ld4(l0 + (j >> 2) * 1024 + (j & 3) * 64);
#pragma unroll
                    for (int ks = 0; ks < 2; ++ks)
#pragma unroll
                        for (int st = 0; st < 2; ++st) of[ks][st] = ld16(o0 + ks * 2048 + st * 1024);
                    float M = mx3(mx3(u2f(le[0]), u2f(le[1]), u2f(le[2])), mx3(u2f(le[3]), u2f(le[4]), u2f(le[5])), mx2(u2f(le[6]), u2f(le[7])));
                    M = max32(max16(M));
                    float e[8];
#pragma unroll
                    for (int j = 0; j < 8; ++j) e[j] = __builtin_amdgcn_exp2f(u2f(le[j]) - M);
                    float Ls = ((e[0] + e[1]) + (e[2] + e[3])) + ((e[4] + e[5]) + (e[6] + e[7]));
                    Ls = red32(Ls, Ls); Ls = red16(Ls, Ls);
                    f4 acc[2][2];
#pragma unroll
                    for (int st = 0; st < 2; ++st)
#pragma unroll
                        for (int ct = 0; ct < 2; ++ct) acc[st][ct] = mfma32(of[1][st], wof[1][ct], mfma32(of[0][st], wof[0][ct], f4{0, 0, 0, 0}));
                    float pc[2];
#pragma unroll
                    for (int ct = 0; ct < 2; ++ct)
                        pc[ct] = ((e[0] * acc[0][ct].x + e[1] * acc[0][ct].y) + (e[2] * acc[0][ct].z + e[3] * acc[0][ct].w)) +
                                 ((e[4] * acc[1][ct].x + e[5] * acc[1][ct].y) + (e[6] * acc[1][ct].z + e[7] * acc[1][ct].w));
                    s = red32(pc[0], pc[1]);
                    s = red16(s, s) * __builtin_amdgcn_rcpf(Ls);
                    if (!anynan(s)) break;
                    bool ok = vok(of[0][0]) && vok(of[0][1]) && vok(of[1][0]) && vok(of[1][1]);
#pragma unroll
                    for (int j = 0; j < 8; ++j) ok = ok && le[j] != INV;
                    if (__all(ok)) break;
                    if (layer == 0 && (it & 7) == 7) bump(true);
                    if (it > SPIN_CAP) break;
                }
                CFENCE();
                if ((q & 1) == 0) *(LAS float*)(L + wa) = s;
            }
            BAR();
            if (layer == 1 && b != 0) { return; }
            {
                const int d0 = w * 128 + n * 8 + (q >> 1) * 4 + (q & 1) * 2;
                float x1a, x1b;
                const f2 xp = *(LAS f2*)(L + L_XS + d0 * 4);
                int o0 = WS_EA + layer * 16384 + q * 1024 + w * 256 + n * 16;
                PIN(o0);
                nap<D_EA1>(); until(2, layer == 0 ? H_EA : C_EA);
                for (int it = 0;; ++it) {
                    CFENCE();
                    v4u e[4];
#pragma unroll
                    for (int i = 0; i < 4; ++i) e[i] = ld16(o0 + i * 4096);
                    float v[8];
#pragma unroll
                    for (int j = 0; j < 4; ++j) {
                        v[2 * j] = dsel(e[3][j], sel_lo, dsel(e[2][j], sel_lo, dsel(e[1][j], sel_lo, bflo(e[0][j]))));
                        v[2 * j + 1] = dsel(e[3][j], sel_hi, dsel(e[2][j], sel_hi, dsel(e[1][j], sel_hi, bfhi(e[0][j]))));
                    }
                    const float z0 = red16(red32(v[0], v[4]), red32(v[2], v[6])), z1 = red16(red32(v[1], v[5]), red32(v[3], v[7]));
                    const unsigned at = pk(z0, z1);
                    const unsigned x1p = pk(xp.x + bflo(at), xp.y + bfhi(at));
                    x1a = bflo(x1p); x1b = bfhi(x1p);
                    if (!anynan(x1a + x1b)) break;
                    if (__all(vok(e[0]) && vok(e[1]) && vok(e[2]) && vok(e[3]))) break;
                    if (layer == 0 && (it & 7) == 7) bump(true);
                    if (it > SPIN_CAP) break;
                }
                CFENCE();
                {
                    const float da = x1a - mu, db = x1b - mu;
                    float s = red32(da + db, da * da + db * db);
                    s = rowsum(red16(s, s));
                    if ((lane & 31) == 0) *(LAS float*)(L + L_ST1 + w * 8 + (lane >> 5) * 4) = s;
                }
                BAR();
                const f4 va = *(LAS f4*)(L + L_ST1), vb = *(LAS f4*)(L + L_ST1 + 16);
                const float S1 = ((va.x + va.z) + (vb.x + vb.z)) * (1.0f / 512.0f), S2 = (va.y + va.w) + (vb.y + vb.w);
                const float mu1 = mu + S1;
                const float var = mx2(fmaf(S2, 1.0f / 512.0f, -S1 * S1), 0.f);
                const float m1 = bfr(mu1);
                const float r1 = __builtin_amdgcn_rcpf(bfr(__builtin_amdgcn_sqrtf(bfr(bfr(var) + 1e-5f))));
                const float m2 = bfr((mu1 - m1) * r1);
                const float r2 = __builtin_amdgcn_rcpf(bfr(__builtin_amdgcn_sqrtf(bfr(bfr(var * r1 * r1) + 1e-5f))));
                const unsigned c1 = pk(x1a - m1, x1b - m1);
                const unsigned h1 = pk(bflo(c1) * r1, bfhi(c1) * r1);
                const unsigned c2 = pk(bflo(h1) - m2, bfhi(h1) - m2);
                const unsigned hb = pk(bflo(c2) * r2, bfhi(c2) * r2);
                const unsigned ta = (hb & 0x7fffu) - (TE0 << 7), tb = ((hb >> 16) & 0x7fffu) - (TE0 << 7);
                const bool ina = ta < 3072u, inb = tb < 3072u;
                const unsigned ia = min(ta, 3071u) + ((hb & 0x8000u) ? 3072u : 0u), ib = min(tb, 3071u) + ((int)hb < 0 ? 3072u : 0u);
                float ya = bflo(*(LAS u16*)(L + L_TAB + ia * 2)), yb = bflo(*(LAS u16*)(L + L_TAB + ib * 2));
                if (__any(!(ina && inb))) {
#pragma nounroll
                    for (int half = 0; half < 2; ++half) {
                        unsigned long long miss = __ballot(half ? !inb : !ina);
                        while (miss) {
                            const int src = __builtin_ctzll(miss);
                            miss &= miss - 1;
                            const unsigned hs = (unsigned)__builtin_amdgcn_readlane((int)hb, src);
                            const float h = half ? bfhi(hs) : bflo(hs);
                            f2 ac2 = {0.f, 0.f};
#pragma unroll 7
                            for (int f = lane; f < NF; f += 64)
                                ac2 = mixp(f2{h, h}, bflo(*(LAS u16*)(L + L_SO + f * 2)), bflo(*(LAS u16*)(L + L_SO + 4096 + f * 2)), bflo(*(LAS u16*)(L + L_SO + 8192 + f * 2)), ac2);
                            float acc = ac2.x;
                            acc = red32(acc, acc); acc = rowsum(red16(acc, acc));
                            const float yy = bfr(bfr(acc) + bflo(a.b2[0]));
                            if (lane == src) { if (half) yb = yy; else ya = yy; }
                        }
                    }
                }
                const unsigned x2p = pk(x1a + ya, x1b + yb);
                if (layer == 1) {
                    *(unsigned*)(a.out + d0) = x2p;
                    return;
                }
                *(LAS unsigned*)(L + L_XB + d0 * 2) = x2p;
                *(LAS f2*)(L + L_XS + d0 * 4) = f2{bflo(x2p), bfhi(x2p)};
                BAR();
                mu = mu1;
            }
        }
    } else if (w < 8) {
        __builtin_amdgcn_s_setprio(3);
        const __amdgpu_buffer_rsrc_t rq = __builtin_amdgcn_make_buffer_rsrc((void*)(a.wq + (size_t)kb * 128 * 4096 + g * 512 + cq * 64), 0, 128 * 8192 - 8064, 0x00020000);
        const int ro_ = lane < 32 ? WS_R + g * 512 + lane * 16 : WS_R + 4096 + (lane & 7) * 128;
        const int ebo = g * 8192 + kb * 2048 + cq * 256;
        v4u x0v = *(const v4u*)(a.x + lane * 8);
        v4u kst[4];
#pragma unroll
        for (int i = 0; i < 4; ++i) kst[i] = __builtin_amdgcn_raw_buffer_load_b128(rq, (w3 * 32 + i * 8 + (lane >> 3)) * 8192 + (lane & 7) * 16, 0, 2);
        v4u xf[4];
#pragma unroll
        for (int ks = 0; ks < 4; ++ks) xf[ks] = *(const v4u*)(a.x + kb * 128 + ks * 32 + q * 8);
        const int ebq = WS_EB + (n == 0 ? 0 : n == 1 ? 2 * 65536 : 65536) + ebo + w3 * 64 + q * 16;
        {
            const v4u iv = {INV, INV, INV, INV};
            if (n < 3) st16(ebq, iv);
            if (w == 4) st16(WS_EC + g * 32768 + (lane >> 2) * 2048 + r * 64 + (lane & 3) * 16, iv);
            if (w == 5) st16(WS_EC + 262144 + g * 32768 + (lane >> 2) * 2048 + r * 64 + (lane & 3) * 16, iv);
            if (w == 6 && lane < 19) st16(lane < 8 ? WS_EA + ((lane >> 2) & 1) * 16384 + (g * 2 + hp) * 1024 + cb * 64 + (lane & 3) * 16
                                        : lane < 16 ? WS_EL + ((lane >> 2) & 1) * 65536 + g * 8192 + (lane & 3) * 2048 + r * 64 : WS_TAB + b * 48 + (lane & 3) * 16, iv);
        }
        BAR();
        asm volatile("" : "+v"(x0v));
        ln_stats(f4{bflo(x0v.x), bfhi(x0v.x), bflo(x0v.y), bfhi(x0v.y)}, f4{bflo(x0v.z), bfhi(x0v.z), bflo(x0v.w), bfhi(x0v.w)});
        asm volatile("" : "+v"(mu), "+v"(rsig));
        SB();
#pragma unroll
        for (int i = 0; i < 4; ++i) { const int row = w3 * 32 + i * 8 + (lane >> 3);
            *(LAS v4u*)(L + L_SQ + row * 128 + (((lane & 7) ^ ((((row >> 1) & 1) | ((row >> 2) & 2)) << 1)) * 16)) = kst[i]; }
        asm volatile("s_waitcnt lgkmcnt(0)" ::: "memory");
        if (lane == 0) __hip_atomic_fetch_add((LAS unsigned*)(L + L_GATE), 1u, __ATOMIC_RELAXED, __HIP_MEMORY_SCOPE_WORKGROUP);
        if (n == 1) { xf[0] = v4u{0x3f803f80u, 0x3f803f80u, 0x3f803f80u, 0x3f803f80u}; xf[1] = xf[0]; xf[2] = xf[0]; xf[3] = xf[0]; }
        asm volatile("" :: "v"(cpf) : "memory");
        while (*gate < 4u) __builtin_amdgcn_s_sleep(1);
        CFENCE();
        v4u wqf[4];
        {
            const int aa = n >> 2, p = n & 3;
#pragma unroll
            for (int ks = 0; ks < 4; ++ks) {
                v2u t[2];
#pragma unroll
                for (int tt = 0; tt < 2; ++tt) { const int row = ks * 32 + q * 8 + tt * 4 + aa, ch = 2 * w3 + (p >> 1);
                    t[tt] = trd(L + L_SQ + row * 128 + ((ch ^ ((((row >> 1) & 1) | ((row >> 2) & 2)) << 1)) * 16) + 8 * (p & 1)); }
                wqf[ks] = v4u{t[0].x, t[0].y, t[1].x, t[1].y};
            }
            f4 c = mfma32(wqf[3], xf[3], mfma32(wqf[2], xf[2], mfma32(wqf[1], xf[1], mfma32(wqf[0], xf[0], f4{0, 0, 0, 0}))));
            const f4 sc = {dpp<0x55>(c.x), dpp<0x55>(c.y), dpp<0x55>(c.z), dpp<0x55>(c.w)};
            if (n == 0) c = (c - sc * mu) * rsig;
            if (n < 2) st16(ebq, __builtin_bit_cast(v4u, c));
            asm volatile("s_waitcnt vmcnt(1)" ::: "memory");
            unsigned arr = 0;
            if (lane == 0) arr = __hip_atomic_fetch_add((LAS unsigned*)(L + L_GATE + 32), 1u, __ATOMIC_RELAXED, __HIP_MEMORY_SCOPE_WORKGROUP);
            if (__builtin_amdgcn_readfirstlane(arr) == 3u) {
                const v2u t = __builtin_bit_cast(v2u, (unsigned long long)__builtin_amdgcn_s_memrealtime());
                if (lane == 0) st16(WS_R + g * 512 + r * 16, v4u{t.x, t.y, t.x, t.y});
            }
        }
        SB();
        f2 csum = {0.f, 0.f};
        auto fetch_eb = [&](int blk) -> f2 {
            int o0 = WS_EB + blk * 65536 + g * 8192 + (lane >> 5) * 2048 + w3 * 512 + (lane & 31) * 16;
            PIN(o0);
            f2 sv;
            if (blk == 1) { nap<D_EB>(); until(2, C_EB); }
            bool ready = blk != 0;
            for (int it = 0;; ++it) {
                CFENCE();
                v4u rec = {0, 0, 0, 0};
                unsigned long long lim = 0;
                if (blk == 0) {
                    lim = __builtin_amdgcn_s_memrealtime() - MARGIN;
                    asm volatile("" : "+s"(lim) :: "memory");
                    SB();
                    if (!ready && lane < 32) rec = ld16(ro_);
                }
                const v4u e0 = ld16(o0), e1 = ld16(o0 + 4096);
                if (blk == 0 && !ready) ready = __all(lane >= 32 || within(rec.x, rec.y, lim) || within(rec.z, rec.w, lim));
                const f4 es = __builtin_bit_cast(f4, e0) + __builtin_bit_cast(f4, e1);
                sv = f2{red32(es.x, es.z), red32(es.y, es.w)};
                if (ready) {
                    if (!anynan(sv.x + sv.y)) break;
                    if (__all(vok(e0) && vok(e1))) break;
                }
                if (blk != 1 && (it & 7) == 7) bump(blk == 2);
                if (it > SPIN_CAP) break;
                if (blk != 1) nap<6>();
            }
            CFENCE();
            return sv;
        };
        const int qa = L_QS + (w3 * 128 + (lane & 31) * 4 + (lane >> 5) * 2) * 2;
        {
            nap<D_G>();
            const f2 qv = fetch_eb(0);
            *(LAS unsigned*)(L + qa) = pk(qv.x, qv.y);
            asm volatile("s_waitcnt lgkmcnt(0)" ::: "memory");
            if (lane == 0) __hip_atomic_fetch_add((LAS unsigned*)(L + L_GATE + 20), 1u, __ATOMIC_RELAXED, __HIP_MEMORY_SCOPE_WORKGROUP);
            if (w == 7 && lane == 1 && r == 0) __builtin_amdgcn_raw_buffer_store_b64(__builtin_bit_cast(v2u, (unsigned long long)__builtin_amdgcn_s_memrealtime()), rs, WS_R + 4096 + g * 128, 0, 16);
            csum = fetch_eb(2);
        }
        const __amdgpu_buffer_rsrc_t ro = __builtin_amdgcn_make_buffer_rsrc((void*)(a.wo + ((size_t)(g * 2 + hp) * 256 + w3 * 64) * 512 + cb * 32), 0, 64 * 1024 - 960, 0x00020000);
        v4u wol[4];
        while (gate[10] == 0u) __builtin_amdgcn_s_sleep(1);
        CFENCE();
#pragma unroll
        for (int i = 0; i < 4; ++i) wol[i] = __builtin_amdgcn_raw_buffer_load_b128(ro, (i * 16 + (lane >> 2)) * 1024 + (lane & 3) * 16, 0, 2);
        SB();
#pragma nounroll
        for (int lyr = 0; lyr < 2; ++lyr) {
            int layer = __builtin_amdgcn_readfirstlane(lyr);
            asm volatile("" : "+s"(layer));
            if (layer == 1) BAR();
            int ea = WS_EC + layer * 262144 + g * 32768 + (w3 >> 1) * 16384 + ((w3 & 1) * 4 + q) * 2048 + r * 64 + (n >> 2) * 16;
            int oa = L_OW + w3 * 528 + lane * 8;
            PIN(ea); PIN(oa);
            if (layer == 0) { while (gate[9] == 0u) __builtin_amdgcn_s_sleep(2);
                while (gate[9] < 4u) {} CFENCE(); } else BAR();
            {
                const f4 ma = *(LAS f4*)(L + L_M + w3 * 32), la = *(LAS f4*)(L + L_M + w3 * 32 + 16);
                f2 v[4];
#pragma unroll
                for (int ww = 0; ww < 4; ++ww) v[ww] = *(LAS f2*)(L + oa + ww * 2112);
                const float M = mx2(mx3(ma.x, ma.y, ma.z), ma.w);
                const float e0 = __builtin_amdgcn_exp2f(ma.x - M), e1 = __builtin_amdgcn_exp2f(ma.y - M), e2 = __builtin_amdgcn_exp2f(ma.z - M), e3 = __builtin_amdgcn_exp2f(ma.w - M);
                const float Ls = (e0 * la.x + e1 * la.y) + (e2 * la.z + e3 * la.w);
                const f2 oo = (v[0] * e0 + v[1] * e1) + (v[2] * e2 + v[3] * e3);
                const float inv = __builtin_amdgcn_rcpf(Ls);
                const unsigned wd = nn(pk(oo.x * inv, oo.y * inv));
                const v4u vv = {wd, dppu<0x55>(wd), dppu<0xAA>(wd), dppu<0xFF>(wd)};
                if ((lane & 3) == 0) st16(ea, vv);
                if (lane == 0) st4(WS_EL + layer * 65536 + g * 8192 + w3 * 2048 + r * 64, f2u(M + __builtin_amdgcn_logf(Ls)));
            }
            if (layer == 0) {
#pragma unroll
                for (int i = 0; i < 4; ++i) { const int row = i * 16 + (lane >> 2);
                    *(LAS v4u*)(L + L_WO + w3 * 4096 + row * 64 + (((lane & 3) ^ (((row >> 3) & 1) << 1)) * 16)) = wol[i]; }
                asm volatile("s_waitcnt lgkmcnt(0)" ::: "memory");
                if (lane == 0) __hip_atomic_fetch_add((LAS unsigned*)(L + L_GATE + 44), 1u, __ATOMIC_RELAXED, __HIP_MEMORY_SCOPE_WORKGROUP);
                if (w == 7) {
                    for (int it = 0;; ++it) {
                        CFENCE();
                        v4u cur = {0, 0, 0, 0};
                        if (lane >= 32) cur = ld16(ro_);
                        if (__all(lane < 32 || within(cur.x, cur.y, __builtin_amdgcn_s_memrealtime()))) break;
                        if ((it & 3) == 3) bump(true);
                        if (it > SPIN_CAP) break;
                        nap<16>();
                    }
                    CFENCE();
                }
            }
            BAR();
            if (w == 4 && lane < 16) {
                f2 v[4];
#pragma unroll
                for (int ww = 0; ww < 4; ++ww) v[ww] = *(LAS f2*)(L + L_WR + ww * 128 + lane * 8);
                const f2 sv = (v[0] + v[1]) + (v[2] + v[3]);
                const unsigned wd = nn(pk(sv.x, sv.y));
                const v4u vv = {wd, dppu<0x55>(wd), dppu<0xAA>(wd), dppu<0xFF>(wd)};
                if ((lane & 3) == 0) st16(WS_EA + layer * 16384 + (g * 2 + hp) * 1024 + cb * 64 + lane * 4, vv);
            }
            if (layer == 1) { return; }
            if (w == 7) gather_hints(WS_EH + 16384 + (lane & 7) * 128, 2);
            until(2, H_TAB);
            for (int it = 0;; ++it) {
                CFENCE();
                const v4u t0 = ld16(WS_TAB + w3 * 3072 + lane * 16), t1 = ld16(WS_TAB + w3 * 3072 + 1024 + lane * 16), t2 = ld16(WS_TAB + w3 * 3072 + 2048 + lane * 16);
                *(LAS v4u*)(L + L_TAB + w3 * 3072 + lane * 16) = t0; *(LAS v4u*)(L + L_TAB + w3 * 3072 + 1024 + lane * 16) = t1; *(LAS v4u*)(L + L_TAB + w3 * 3072 + 2048 + lane * 16) = t2;
                if (__all(vok(t0) && vok(t1) && vok(t2))) break;
                if ((it & 7) == 7) bump(true);
                if (it > SPIN_CAP) break;
            }
            CFENCE();
            int qo = WS_EB + 65536 + ebo + w3 * 64 + q * 16;
            int xa_ = L_XB + kb * 256 + q * 16;
            PIN(qo); PIN(xa_);
            BAR();
            BAR();
            {
                v4u xa[4];
#pragma unroll
                for (int ks = 0; ks < 4; ++ks) xa[ks] = *(LAS v4u*)(L + xa_ + ks * 64);
                const f4 c = mfma32(wqf[3], xa[3], mfma32(wqf[1], xa[1], f4{0, 0, 0, 0})) + mfma32(wqf[2], xa[2], mfma32(wqf[0], xa[0], f4{0, 0, 0, 0}));
                if (n == 0) st16(qo, __builtin_bit_cast(v4u, c));
            }
            SB();
            ln_stats(*(LAS f4*)(L + L_XS + lane * 32), *(LAS f4*)(L + L_XS + lane * 32 + 16));
            csum *= mu;
            {
                const f2 qv = (fetch_eb(1) - csum) * rsig;
                *(LAS unsigned*)(L + qa) = pk(qv.x, qv.y);
            }
        }
    } else {
        v4u pl[4];
        if (w < 11) {
            const __amdgpu_buffer_rsrc_t rp = __builtin_amdgcn_make_buffer_rsrc((void*)(w == 8 ? a.w1 : w == 9 ? a.b1 : a.w2), 0, NF * 2, 0x00020000);
#pragma unroll
            for (int i = 0; i < 4; ++i) pl[i] = __builtin_amdgcn_raw_buffer_load_b128(rp, i * 1024 + lane * 16, 0, 0);
        }
        BAR();
        const float b2v = bflo(a.b2[0]);
        if (w < 11) {
#pragma unroll
            for (int i = 0; i < 4; ++i) *(LAS v4u*)(L + L_SO + w3 * 4096 + i * 1024 + lane * 16) = pl[i];
        }
        asm volatile("s_waitcnt lgkmcnt(0)" ::: "memory");
        if (lane == 0) __hip_atomic_fetch_add((LAS unsigned*)(L + L_GATE + 24), 1u, __ATOMIC_RELAXED, __HIP_MEMORY_SCOPE_WORKGROUP);
        while (gate[6] < 4u) __builtin_amdgcn_s_sleep(1);
        asm volatile("s_waitcnt vmcnt(0)" ::: "memory");
        asm volatile("" :: "v"(cpf) : "memory");
        float w1f[7], b1f[7], w2f[7];
#pragma unroll
        for (int j = 0; j < 7; ++j) { const int f = ((tid & 255) + 256 * j) * 2;
            w1f[j] = bflo(*(LAS u16*)(L + L_SO + f)); b1f[j] = bflo(*(LAS u16*)(L + L_SO + 4096 + f)); w2f[j] = bflo(*(LAS u16*)(L + L_SO + 8192 + f)); }
        const unsigned hbits0 = (unsigned)((b >> 7) << 15) | (unsigned)((TE0 << 7) + (b & 127) * 24);
        float t12[12];
#pragma unroll
        for (int e = 0; e < 12; ++e) {
            const f2 h = {u2f((hbits0 + e) << 16), u2f((hbits0 + e + 12) << 16)};
            f2 acc = {0.f, 0.f};
#pragma unroll
            for (int j = 0; j < 7; ++j) acc = mixp(h, w1f[j], b1f[j], w2f[j], acc);
            t12[e] = red32(acc.x, acc.y);
        }
#pragma unroll
        for (int j = 0; j < 6; ++j) { const float v = rowsum(red16(t12[j], t12[j + 6])); if (n == 0) *(LAS float*)(L + L_TR + w3 * 96 + q * 24 + j * 4) = v; }
        asm volatile("s_waitcnt lgkmcnt(0)" ::: "memory");
        unsigned arr = 0;
        if (lane == 0) arr = __hip_atomic_fetch_add((LAS unsigned*)(L + L_GATE + 28), 1u, __ATOMIC_RELAXED, __HIP_MEMORY_SCOPE_WORKGROUP);
        if (__builtin_amdgcn_readfirstlane(arr) == 3u) {
            while (gate[8] < 4u) __builtin_amdgcn_s_sleep(1);
            CFENCE();
            if (lane < 12) {
                f2 tv[4];
#pragma unroll
                for (int ww = 0; ww < 4; ++ww) tv[ww] = *(volatile LAS f2*)(L + L_TR + ww * 96 + lane * 8);
                const f2 sv = (tv[0] + tv[1]) + (tv[2] + tv[3]);
                const unsigned wd = nn(pk(bfr(sv.x) + b2v, bfr(sv.y) + b2v));
                const v4u vv = {wd, dppu<0x55>(wd), dppu<0xAA>(wd), dppu<0xFF>(wd)};
                if ((lane & 3) == 0) st16(WS_TAB + b * 48 + lane * 4, vv);
            }
        }
    }
}

}

extern "C" void kernel_launch(void* const* d_in, const int* in_sizes, int n_in,
                              void* d_out, int out_size, void* d_ws, size_t ws_size,
                              hipStream_t stream) {
    fz::Args a;
    a.x = (const u16*)d_in[0]; a.kc = (const u16*)d_in[1]; a.vc = (const u16*)d_in[2];
    a.wq = (const u16*)d_in[3]; a.wo = (const u16*)d_in[4];
    a.w1 = (const u16*)d_in[6]; a.b1 = (const u16*)d_in[7]; a.w2 = (const u16*)d_in[8]; a.b2 = (const u16*)d_in[9];
    a.out = (u16*)d_out; a.ws = (unsigned char*)d_ws;
    (void)hipFuncSetAttribute((const void*)fz::fused, hipFuncAttributeMaxDynamicSharedMemorySize, fz::LDS_BYTES);
    fz::fused<<<256, 768, fz::LDS_BYTES, stream>>>(a);
}
```
